```python
import torch

def _softmax(x, axis=-1):
    return torch.softmax(x, dim=axis)
def _gelu(x):
    return torch.nn.functional.gelu(x, approximate="tanh")
def _silu(x):
    return torch.nn.functional.silu(x)
def _sigmoid(x):
    return torch.sigmoid(x)
def _layernorm(x, axis=-1, eps=1e-5):
    m = x.mean(dim=axis, keepdim=True)
    v = x.var(dim=axis, keepdim=True, unbiased=False)
    return (x - m) / torch.sqrt(v + eps)
def _scatter_add(tgt, idx, src, axis=0):
    return tgt.clone().index_add_(axis, idx, src)
def _segment_sum(src, seg, n):
    out = torch.zeros((n,) + src.shape[1:], dtype=src.dtype)
    return out.index_add_(0, seg, src)
def _pad(x, pw):
    flat = tuple(v for pair in reversed(pw) for v in pair)
    return torch.nn.functional.pad(x, flat)

kernel_name = 'sequential_preLN__sliding_block_cached__dense_activation_silu__decode'

def setup_inputs():

    _w = (lambda a: (lambda t: t.to(torch.bfloat16) if t.is_floating_point() else t)(torch.as_tensor(a)))
    def _A_setup():
        B, Sq, D = 1, 1, 576
        Hq, Hkv, Sk, Dh = 32, 16, 8192, 128
        assert Hq % Hkv == 0, (Hq, Hkv)
        return {
            "x": _w((lambda *s: torch.randn(*s, dtype=torch.bfloat16) * 0.05)(B, Sq, D)),
            "k_cache": _w((lambda *s: torch.randn(*s, dtype=torch.bfloat16) * 3.2)(B, Hkv, Sk, Dh)),
            "v_cache": _w((lambda *s: torch.randn(*s, dtype=torch.bfloat16) * 0.8)(B, Hkv, Sk, Dh)),
            "wq": _w((lambda *s: torch.randn(*s, dtype=torch.bfloat16) * 3.2)(D, Hq * Dh) * (D ** -0.5)),
            "wo": _w((lambda *s: torch.randn(*s, dtype=torch.bfloat16) * 0.8)(Hq * Dh, D) * ((Hq * Dh) ** -0.5)),
        }
    _A_in = _A_setup()

    def _B_setup():
        B, S, D, F = 1, 1, 576, 4608
        return {
            "x": _w((lambda *s: torch.randn(*s, dtype=torch.bfloat16) * 0.05)(B, S, D)),
            "w1": _w((lambda *s: torch.randn(*s, dtype=torch.bfloat16) * 3.2)(D, F) * (D ** -0.5)),
            "w2": _w((lambda *s: torch.randn(*s, dtype=torch.bfloat16) * 0.4)(F, D) * (F ** -0.5)),
        }
    _B_in = _B_setup()

    return {
        "A_x": _A_in["x"],
        "A_k_cache": _A_in["k_cache"],
        "A_v_cache": _A_in["v_cache"],
        "A_wq": _A_in["wq"],
        "A_wo": _A_in["wo"],
        "B_x": _B_in["x"],
        "B_w1": _B_in["w1"],
        "B_w2": _B_in["w2"],
    }

def reference(A_x, A_k_cache, A_v_cache, A_wq, A_wo, B_x, B_w1, B_w2):
    def _A_ref(x, k_cache, v_cache, wq, wo):
        B, Sq, D = x.shape
        Hq, Dh = 32, 128
        Hkv, Sk = k_cache.shape[1], k_cache.shape[2]
        W = min(4096, max(256, Sk // 4))
        rep = Hq // Hkv
        q = (lambda x, *d: x.permute(*d))((x @ wq).reshape(B, Sq, Hq, Dh), 0, 2, 1, 3)
        k = (lambda x, n, axis: torch.repeat_interleave(x, n, dim=axis))(k_cache[:, :, Sk - W:, :], rep, axis=1)
        v = (lambda x, n, axis: torch.repeat_interleave(x, n, dim=axis))(v_cache[:, :, Sk - W:, :], rep, axis=1)
        scores = torch.einsum("bhqd,bhkd->bhqk", q, k) * (Dh ** -0.5)
        p = _softmax(scores, axis=-1)
        o = torch.einsum("bhqk,bhkd->bhqd", p, v)
        return (lambda x, *d: x.permute(*d))(o, 0, 2, 1, 3).reshape(B, Sq, Hq * Dh) @ wo

    def _B_ref(x, w1, w2):
        h = torch.einsum("bsd,df->bsf", x, w1)
        h = _silu(h)
        out = torch.einsum("bsf,fd->bsd", h, w2)
        return out

    x = A_x
    h = x + _A_ref(_layernorm(x, axis=-1), A_k_cache, A_v_cache, A_wq, A_wo)
    h2 = h + _B_ref(_layernorm(h, axis=-1), B_w1, B_w2)
    return h2
```

```cpp
#include <hip/hip_runtime.h>
#include <hip/hip_bf16.h>
#include <stdint.h>

constexpr int D = 576, HQ = 32, HKV = 16, SK = 8192, DH = 128, W = 2048, F = 4608;

__device__ __forceinline__ float bf2f(unsigned short b) { return __uint_as_float(((unsigned)b) << 16); }
__device__ __forceinline__ unsigned short f2bf(float f) { unsigned u = __float_as_uint(f); u += 0x7FFFu + ((u >> 16) & 1u); return (unsigned short)(u >> 16); }
__device__ __forceinline__ float rbf(float f) { return bf2f(f2bf(f)); }

#define MIMIC 0
__device__ __forceinline__ float R(float f) { return MIMIC ? rbf(f) : f; }

__device__ void layernorm_block(const float* in, float* out, float* red) {
  int tid = threadIdx.x;
  if (tid == 0) {
    float s = 0; for (int i = 0; i < D; i++) s += in[i];
    float m = R(s / D);
    float mean = s / D;
    float v = 0; for (int i = 0; i < D; i++) { float d = in[i] - mean; v += d * d; }
    v = R(v / D);
    float den = R(sqrtf(R(v + 1e-5f)));
    red[0] = m; red[1] = den;
  }
  __syncthreads();
  for (int i = tid; i < D; i += blockDim.x) out[i] = R(R(in[i] - red[0]) / red[1]);
  __syncthreads();
}

__global__ void k_q(const unsigned short* x, const unsigned short* wq, float* q) {
  __shared__ float xs[D], xn[D], red[2];
  for (int i = threadIdx.x; i < D; i += blockDim.x) xs[i] = bf2f(x[i]);
  __syncthreads();
  layernorm_block(xs, xn, red);
  int col = blockIdx.x * blockDim.x + threadIdx.x;
  float acc = 0;
  for (int i = 0; i < D; i++) acc += xn[i] * bf2f(wq[(size_t)i * 4096 + col]);
  q[col] = R(acc);
}

__global__ void k_attn(const float* q, const unsigned short* kc, const unsigned short* vc, float* o) {
  __shared__ float sc[W]; __shared__ float qs[DH]; __shared__ float red[256];
  int h = blockIdx.x, g = h / 2, tid = threadIdx.x;
  if (tid < DH) qs[tid] = q[h * DH + tid];
  __syncthreads();
  const unsigned short* K = kc + ((size_t)g * SK + (SK - W)) * DH;
  const unsigned short* V = vc + ((size_t)g * SK + (SK - W)) * DH;
  float mx = -INFINITY;
  for (int j = tid; j < W; j += 256) {
    float a = 0; for (int d = 0; d < DH; d++) a += qs[d] * bf2f(K[(size_t)j * DH + d]);
    a = R(R(a) * 0.08838834764831845f);
    sc[j] = a; mx = fmaxf(mx, a);
  }
  red[tid] = mx; __syncthreads();
  for (int s = 128; s > 0; s >>= 1) { if (tid < s) red[tid] = fmaxf(red[tid], red[tid + s]); __syncthreads(); }
  mx = red[0]; __syncthreads();
  float sum = 0;
  for (int j = tid; j < W; j += 256) { float e = expf(sc[j] - mx); sc[j] = e; sum += e; }
  red[tid] = sum; __syncthreads();
  for (int s = 128; s > 0; s >>= 1) { if (tid < s) red[tid] += red[tid + s]; __syncthreads(); }
  sum = red[0]; __syncthreads();
  for (int j = tid; j < W; j += 256) sc[j] = R(sc[j] / sum);
  __syncthreads();
  if (tid < DH) {
    float a = 0; for (int j = 0; j < W; j++) a += sc[j] * bf2f(V[(size_t)j * DH + tid]);
    o[h * DH + tid] = R(a);
  }
}

__global__ void k_wo(const float* o, const unsigned short* wo, const unsigned short* x, float* h) {
  int col = blockIdx.x * blockDim.x + threadIdx.x;
  if (col >= D) return;
  float acc = 0;
  for (int i = 0; i < 4096; i++) acc += o[i] * bf2f(wo[(size_t)i * D + col]);
  h[col] = R(bf2f(x[col]) + R(acc));
}

__global__ void k_w1(const float* h, const unsigned short* w1, float* g) {
  __shared__ float xs[D], xn[D], red[2];
  for (int i = threadIdx.x; i < D; i += blockDim.x) xs[i] = h[i];
  __syncthreads();
  layernorm_block(xs, xn, red);
  int col = blockIdx.x * blockDim.x + threadIdx.x;
  float acc = 0;
  for (int i = 0; i < D; i++) acc += xn[i] * bf2f(w1[(size_t)i * F + col]);
  acc = R(acc);
  g[col] = R(acc / (1.0f + expf(-acc)));
}

__global__ void k_w2(const float* g, const unsigned short* w2, const float* h, unsigned short* out) {
  int col = blockIdx.x * blockDim.x + threadIdx.x;
  if (col >= D) return;
  float acc = 0;
  for (int i = 0; i < F; i++) acc += g[i] * bf2f(w2[(size_t)i * D + col]);
  out[col] = f2bf(h[col] + R(acc));
}

extern "C" void kernel_launch(void* const* d_in, const int* in_sizes, int n_in,
                              void* d_out, int out_size, void* d_ws, size_t ws_size,
                              hipStream_t stream) {
  const unsigned short* x = (const unsigned short*)d_in[0];
  const unsigned short* kc = (const unsigned short*)d_in[1];
  const unsigned short* vc = (const unsigned short*)d_in[2];
  const unsigned short* wq = (const unsigned short*)d_in[3];
  const unsigned short* wo = (const unsigned short*)d_in[4];
  const unsigned short* w1 = (const unsigned short*)d_in[6];
  const unsigned short* w2 = (const unsigned short*)d_in[7];
  float* ws = (float*)d_ws;
  float* q = ws; float* o = ws + 4096; float* h = ws + 8192; float* g = ws + 9216;
  k_q<<<16, 256, 0, stream>>>(x, wq, q);
  k_attn<<<32, 256, 0, stream>>>(q, kc, vc, o);
  k_wo<<<9, 64, 0, stream>>>(o, wo, x, h);
  k_w1<<<18, 256, 0, stream>>>(h, w1, g);
  k_w2<<<9, 64, 0, stream>>>(g, w2, h, (unsigned short*)d_out);
}
```

```cpp
#include <hip/hip_runtime.h>
#include <stdint.h>
#include <type_traits>

namespace {

#define LAS __attribute__((address_space(3)))
typedef unsigned int u32;
typedef u32 v4u __attribute__((ext_vector_type(4)));
typedef u32 v2u __attribute__((ext_vector_type(2)));
typedef float v4f __attribute__((ext_vector_type(4)));
typedef float v2f __attribute__((ext_vector_type(2)));
typedef short v4s __attribute__((ext_vector_type(4)));
typedef short v8s __attribute__((ext_vector_type(8)));
typedef __bf16 v8b __attribute__((ext_vector_type(8)));
typedef __bf16 v2b __attribute__((ext_vector_type(2)));
typedef __amdgpu_buffer_rsrc_t rsrc_t;

#define WIN 10
#ifndef NV1
#define NV1 4
#endif
#define BULK_AUX 1

constexpr u32 L_WQ = 0, L_K = 18432, L_V = 51200, L_S = 83968, L_FL = 157696;
constexpr u32 L_XN = L_FL + 128, L_SC = L_XN + 1152, L_Y = L_SC + 1024, L_END = 163840;
constexpr u32 L_QST = 0, L_QV = 8192, L_P = L_QV + 8 * 576;
constexpr u32 F_WQ = L_FL, F_Q = L_FL + 16, F_V = L_FL + 32, F_S = L_FL + 48, F_QD = L_FL + 64, F_SC = L_FL + 80, F_TAG = L_FL + 96;
constexpr u32 L_XR = L_Y, L_ST = L_Y + 1152, L_H = L_ST + 128, L_XN2 = L_H + 1152;
constexpr u32 L_VEC = L_Y, L_PART = L_Y + 1152;
static_assert(L_XN2 + 9 * 128 <= L_END && L_PART + 9 * 256 <= L_END, "lds");
constexpr u32 WS_EP = 0, WS_QP = 4096, WS_OP = WS_QP + 16 * 8192, OPS = 1152, WS_HP = WS_OP + 256 * OPS;
constexpr u32 WS_PP = WS_HP + 8 * 2304, WS_FP = WS_PP + 72 * 2304, WS_END = WS_FP + 8 * 2304;
constexpr u32 SPIN_MAX = 1u << 16;

struct Args {
  const void* x; const void* kc; const void* vc; const void* wq; const void* wo; const void* w1; const void* w2;
  void* out; void* ws;
};

template <int N, class F> __device__ __forceinline__ void static_for(F&& f) {
  if constexpr (N > 0) { static_for<N - 1>(f); f(std::integral_constant<int, N - 1>{}); }
}

__device__ __forceinline__ float bf_lo(u32 u) { return __uint_as_float(u << 16); }
__device__ __forceinline__ float bf_hi(u32 u) { return __uint_as_float(u & 0xffff0000u); }
__device__ __forceinline__ u32 pk_bf(float lo, float hi) {
  v2b r = __builtin_convertvector((v2f){lo, hi}, v2b);
  return __builtin_bit_cast(u32, r);
}
__device__ __forceinline__ float rbf(float f) { return bf_lo(pk_bf(f, f)); }

template <int C> __device__ __forceinline__ float dppf(float v) {
  return __builtin_bit_cast(float, __builtin_amdgcn_update_dpp(0, __builtin_bit_cast(int, v), C, 0xf, 0xf, true));
}
__device__ __forceinline__ float r16_sum(float v) { v += dppf<0xB1>(v); v += dppf<0x4E>(v); v += dppf<0x141>(v); v += dppf<0x140>(v); return v; }
__device__ __forceinline__ float r16_max(float v) { v = fmaxf(v, dppf<0xB1>(v)); v = fmaxf(v, dppf<0x4E>(v)); v = fmaxf(v, dppf<0x141>(v)); v = fmaxf(v, dppf<0x140>(v)); return v; }
__device__ __forceinline__ float x16_sum(float v) { auto r = __builtin_amdgcn_permlane16_swap(__float_as_uint(v), __float_as_uint(v), false, false); return __uint_as_float(r[0]) + __uint_as_float(r[1]); }
__device__ __forceinline__ float x32_sum(float v) { auto r = __builtin_amdgcn_permlane32_swap(__float_as_uint(v), __float_as_uint(v), false, false); return __uint_as_float(r[0]) + __uint_as_float(r[1]); }
__device__ __forceinline__ float x16_max(float v) { auto r = __builtin_amdgcn_permlane16_swap(__float_as_uint(v), __float_as_uint(v), false, false); return fmaxf(__uint_as_float(r[0]), __uint_as_float(r[1])); }
__device__ __forceinline__ float x32_max(float v) { auto r = __builtin_amdgcn_permlane32_swap(__float_as_uint(v), __float_as_uint(v), false, false); return fmaxf(__uint_as_float(r[0]), __uint_as_float(r[1])); }

template <class T> __device__ __forceinline__ T lds_ld(u32 a) { return *(const LAS T*)(size_t)a; }
template <class T> __device__ __forceinline__ void lds_st(u32 a, T v) { *(LAS T*)(size_t)a = v; }
__device__ __forceinline__ v4s lds_tr(u32 a) { return __builtin_amdgcn_ds_read_tr16_b64_v4i16((LAS v4s*)(size_t)a); }
__device__ __forceinline__ v8b cat8(v4s a, v4s b) { return __builtin_bit_cast(v8b, __builtin_shufflevector(a, b, 0, 1, 2, 3, 4, 5, 6, 7)); }
__device__ __forceinline__ v4f mfma32(v8b a, v8b b, v4f c) { return __builtin_amdgcn_mfma_f32_16x16x32_bf16(a, b, c, 0, 0, 0); }

__device__ __forceinline__ void lgkm0() { asm volatile("s_waitcnt lgkmcnt(0)" ::: "memory"); }
template <int N> __device__ __forceinline__ void vmwait() { asm volatile("s_waitcnt vmcnt(%0)" ::"n"(N) : "memory"); }
__device__ __forceinline__ void cbar() { asm volatile("" ::: "memory"); }
__device__ __forceinline__ void wg_barrier() { lgkm0(); __builtin_amdgcn_s_barrier(); cbar(); }

template <int AUX> __device__ __forceinline__ void dma(rsrc_t r, u32 voff, u32 soff, u32 lds) {
  if constexpr (AUX == 1) asm volatile("s_mov_b32 m0, %0\n\ts_nop 0\n\tbuffer_load_dwordx4 %1, %2, %3 offen nt lds" ::"s"(lds), "v"(voff), "s"(r), "s"(soff) : "memory");
  else if constexpr (AUX == 16) asm volatile("s_mov_b32 m0, %0\n\ts_nop 0\n\tbuffer_load_dwordx4 %1, %2, %3 offen sc1 lds" ::"s"(lds), "v"(voff), "s"(r), "s"(soff) : "memory");
  else asm volatile("s_mov_b32 m0, %0\n\ts_nop 0\n\tbuffer_load_dwordx4 %1, %2, %3 offen lds" ::"s"(lds), "v"(voff), "s"(r), "s"(soff) : "memory");
}

__device__ __forceinline__ void set_flag(u32 a, int lane) {
  if (lane == 0) *(volatile LAS u32*)(size_t)a = 1u;
}
__device__ __forceinline__ void wait_flag4(u32 a) {
#pragma unroll 1
  for (u32 it = 0; it < (1u << 22); ++it) {
    v4u v;
    asm volatile("ds_read_b128 %0, %1\n\ts_waitcnt lgkmcnt(0)" : "=v"(v) : "v"(a) : "memory");
    if (__builtin_amdgcn_readfirstlane(v.x & v.y & v.z & v.w)) break;
    __builtin_amdgcn_s_sleep(1);
  }
}
__device__ __forceinline__ void wait_cnt(u32 a, u32 n) {
#pragma unroll 1
  for (u32 it = 0; it < (1u << 22); ++it) {
    u32 v;
    asm volatile("ds_read_b32 %0, %1\n\ts_waitcnt lgkmcnt(0)" : "=v"(v) : "v"(a) : "memory");
    if (__builtin_amdgcn_readfirstlane(v) >= n) break;
    __builtin_amdgcn_s_sleep(1);
  }
}

__device__ __forceinline__ v4u gld(rsrc_t r, u32 off) { return __builtin_amdgcn_raw_buffer_load_b128(r, off, 0, 16); }
__device__ __forceinline__ void gst(rsrc_t r, u32 off, v4u v) { __builtin_amdgcn_raw_buffer_store_b128(v, r, off, 0, 16); }
__device__ __forceinline__ v4u pack4(v4f v, u32 t) {
  v4u r;
#pragma unroll
  for (int i = 0; i < 4; ++i) r[i] = ((__float_as_uint(v[i]) + 0x200u) & ~0x3ffu) | t;
  return r;
}
__device__ __forceinline__ v4f unpack4(v4u w, u32 t, u32& bad) {
  v4u y = w ^ t;
  bad |= y.x | y.y | y.z | y.w;
  return __builtin_bit_cast(v4f, y);
}
__device__ __forceinline__ bool all_ok(u32 bad) { return __all((bad & 0x3ffu) == 0u); }

#define STAMP(i) do { } while (0)
#define STAMP_DECL do { } while (0)
#define STAMP_FLUSH() do { } while (0)

__device__ __forceinline__ u32 t128_tr(int lane, int t, int nb) {
  const int g = lane >> 4, q = (lane & 15) >> 2, p = lane & 3;
  const int r = 8 * g + 4 * t + q;
  const int f = (q >> 1) | (((t ^ g) & 1) << 1);
  return (u32)(r * 128 + ((nb ^ f) << 5) + ((p >> 1) << 4) + ((p & 1) << 3));
}
__device__ __forceinline__ u32 off_b(u32 row, u32 ch) { return 256u * row + 16u * (ch ^ (((row & 3) << 2) | ((row >> 2) & 3))); }

template <int NP> __device__ __forceinline__ v4f gather_rows(rsrc_t rws, u32 base, int lane, u32 t) {
  const int pq = lane >> 4, q = lane & 15;
  const u32 o = base + pq * 2304 + q * 16;
  v4f s;
#pragma unroll 1
  for (u32 it = 0;; ++it) {
    cbar();
    u32 bad = 0;
    v4u w0 = gld(rws, o), w1 = gld(rws, o + 4 * 2304), w2;
    if constexpr (NP == 9) w2 = gld(rws, base + 8 * 2304 + q * 16);
    s = unpack4(w0, t, bad) + unpack4(w1, t, bad);
    if constexpr (NP == 9) { v4f e = unpack4(w2, t, bad); if (pq == 0) s += e; }
    if (all_ok(bad) || it >= SPIN_MAX) break;
  }
#pragma unroll
  for (int i = 0; i < 4; ++i) s[i] = x32_sum(x16_sum(s[i]));
  return s;
}

__device__ __forceinline__ v4f pick4(v4f a0, v4f a1, v4f a2, v4f a3, int c16) {
  const bool b0 = c16 & 1, b1 = c16 & 2;
  v4f r;
#pragma unroll
  for (int i = 0; i < 4; ++i) {
    const float lo = b0 ? a1[i] : a0[i], hi = b0 ? a3[i] : a2[i];
    r[i] = b1 ? hi : lo;
  }
  return r;
}

__global__ void __launch_bounds__(768, 3) fused_decode_kernel(Args a) {
  extern __shared__ __attribute__((aligned(16))) unsigned char smem[];
  const int tid = threadIdx.x, lane = tid & 63;
  const int wv = __builtin_amdgcn_readfirstlane(tid >> 6);
  const int b = blockIdx.x, x = b & 7, j = b >> 3;
  const int g = 2 * x + (j >> 4), s = j & 15;
  const int role = j < 9 ? 0 : (j < 18 ? 1 : (j < 27 ? 2 : 3));
  const int jj = j - 9 * role;
  const rsrc_t rws = __builtin_amdgcn_make_buffer_rsrc(a.ws, 0, WS_END, 0x00020000);
  STAMP_DECL;
  STAMP(0);
  int cw;
  u32 tag32;

  if (wv < 4) {
    const int lw = wv;
    constexpr int W = WIN;
    constexpr int N_WQ = 5, N_K = 13, N_V1 = 13 + NV1, N_QG = N_V1 + 2, N_V2 = 23;
    auto after = [&](auto n_) {
      constexpr int n = decltype(n_)::value;
      if constexpr (n > W) vmwait<W>();
      if constexpr (n - W == N_WQ) set_flag(F_WQ + 4 * lw, lane);
      if constexpr (n - W == N_QG) set_flag(F_Q + 4 * lw, lane);
      if constexpr (n - W == N_V2) set_flag(F_V + 4 * lw, lane);
    };
    const u32 vt = (u32)(((lane & 7) ^ (((lane >> 4) & 1) << 1) ^ (((lane >> 5) & 1) << 2)) * 16);
    {
      const rsrc_t rq = __builtin_amdgcn_make_buffer_rsrc((void*)a.wq, 0, 576 * 4096 * 2, 0x00020000);
      const u32 voff = (u32)(lane >> 3) * 8192u + vt;
      const u32 base = (u32)(s >> 2) * 144u * 8192u + (u32)(g * 256 + (s & 3) * 64) * 2u;
      static_for<5>([&](auto k_) {
        constexpr int k = decltype(k_)::value;
        const int i = min(lw + 4 * k, 17);
        dma<BULK_AUX>(rq, voff ^ (u32)((i & 1) << 6), base + i * 65536u, L_WQ + i * 1024u);
        after(std::integral_constant<int, k + 1>{});
      });
    }
    __builtin_amdgcn_s_barrier();
    cbar();
    const u32 vkv = ((u32)(lane >> 4) * 256u + (u32)(((lane & 15) ^ ((lane >> 4) << 2)) * 16)) ^ (u32)(lw << 4);
    const u32 bkv = (u32)(g * 8192 + 6144 + s * 128) * 256u + lw * 1024u;
    const rsrc_t rk = __builtin_amdgcn_make_buffer_rsrc((void*)a.kc, 0, 16 * 8192 * 256, 0x00020000);
    const rsrc_t rv = __builtin_amdgcn_make_buffer_rsrc((void*)a.vc, 0, 16 * 8192 * 256, 0x00020000);
    static_for<8>([&](auto k_) {
      constexpr int k = decltype(k_)::value;
      dma<BULK_AUX>(rk, vkv, bkv + k * 4096u, L_K + lw * 1024u + k * 4096u);
      after(std::integral_constant<int, N_WQ + k + 1>{});
    });
    static_for<NV1>([&](auto k_) {
      constexpr int k = decltype(k_)::value;
      dma<BULK_AUX>(rv, vkv, bkv + k * 4096u, L_V + lw * 1024u + k * 4096u);
      after(std::integral_constant<int, N_K + k + 1>{});
    });
    vmwait<NV1>();
    if constexpr (N_V1 - W < N_WQ) set_flag(F_WQ + 4 * lw, lane);
    STAMP(1);
    wait_flag4(F_QD);
    static_for<2>([&](auto k_) {
      constexpr int k = decltype(k_)::value;
      dma<16>(rws, (u32)lane * 16u, WS_QP + g * 8192u + (lw + 4 * k) * 1024u, L_QST + (lw + 4 * k) * 1024u);
      after(std::integral_constant<int, N_V1 + k + 1>{});
    });
    static_for<8 - NV1>([&](auto k_) {
      constexpr int k = decltype(k_)::value;
      dma<BULK_AUX>(rv, vkv, bkv + (NV1 + k) * 4096u, L_V + lw * 1024u + (NV1 + k) * 4096u);
      after(std::integral_constant<int, N_QG + k + 1>{});
    });
    if (role < 3) {
      const void* wp = role == 0 ? a.wo : (role == 1 ? a.w1 : a.w2);
      const u32 wbytes = role == 0 ? 4096u * 576u * 2u : 4608u * 576u * 2u;
      const rsrc_t rw = __builtin_amdgcn_make_buffer_rsrc((void*)wp, 0, wbytes, 0x00020000);
      const u32 rs = role == 1 ? 9216u : 1152u;
      const u32 base = role == 0 ? (u32)x * 512u * 1152u + (u32)jj * 128u
                     : role == 1 ? (u32)jj * 64u * 9216u + (u32)x * 1152u
                                 : (u32)x * 576u * 1152u + (u32)jj * 128u;
      const int nk = role == 0 ? 16 : 18;
      const u32 voff = (u32)(lane >> 3) * rs + vt;
      static_for<18>([&](auto k_) {
        constexpr int k = decltype(k_)::value;
        if (k < nk) {
          const u32 i = lw + 4 * k;
          const u32 rb = role == 1 ? (i * 7282u) >> 16 : i;
          const u32 p = role == 1 ? i - rb * 9u : 0u;
          dma<BULK_AUX>(rw, voff ^ ((rb & 1u) << 6), base + rb * 8u * rs + p * 128u, L_S + (p * 8u + rb) * 1024u);
          after(std::integral_constant<int, N_V2 + k + 1>{});
        }
      });
      static_assert(N_V2 + W <= N_V2 + 16, "flags must be inside the guarded range");
    } else {
      if constexpr (N_V2 - W < N_QG) { vmwait<8 - NV1>(); set_flag(F_Q + 4 * lw, lane); }
      vmwait<0>();
      set_flag(F_V + 4 * lw, lane);
    }
    vmwait<0>();
    set_flag(F_S + 4 * lw, lane);
    STAMP(2);
    if (lw != 0 || role == 0 || role == 3) { STAMP_FLUSH(); return; }
    lgkm0();
    tag32 = lds_ld<u32>(F_TAG);
    cw = 8;
  } else {
    cw = wv - 4;
    const u32 E = __builtin_amdgcn_raw_buffer_load_b32(rws, WS_EP, 0, 16);
    v4u xa, xb;
    if (cw < 4) {
      const rsrc_t rx = __builtin_amdgcn_make_buffer_rsrc((void*)a.x, 0, 1152, 0x00020000);
      xa = __builtin_amdgcn_raw_buffer_load_b128(rx, lane * 16, 0, 0);
      xb = __builtin_amdgcn_raw_buffer_load_b128(rx, 1024 + lane * 16, 0, 0);
    }
    if (cw == 7 && lane < 32) lds_st<u32>(L_FL + lane * 4, 0u);
    wg_barrier();
    tag32 = __builtin_amdgcn_readfirstlane(E) + 1u;
    const int g16 = lane >> 4, c16 = lane & 15;
    if (cw < 4) {
      float v[16];
#pragma unroll
      for (int i = 0; i < 4; ++i) { v[2 * i] = bf_lo(xa[i]); v[2 * i + 1] = bf_hi(xa[i]); v[8 + 2 * i] = bf_lo(xb[i]); v[9 + 2 * i] = bf_hi(xb[i]); }
      float sm = 0.f;
#pragma unroll
      for (int i = 0; i < 16; ++i) sm += v[i];
      sm = x32_sum(x16_sum(r16_sum(sm)));
      const float mean = sm * (1.0f / 576.0f);
      float s1 = 0.f, s2 = 0.f;
#pragma unroll
      for (int i = 0; i < 8; ++i) { const float d = v[i] - mean, e = v[8 + i] - mean; s1 += d * d; s2 += e * e; }
      if (lane < 8) s1 += s2;
      s1 = x32_sum(x16_sum(r16_sum(s1)));
      const float m = rbf(mean);
      const float den = rbf(__builtin_amdgcn_sqrtf(rbf(rbf(s1 * (1.0f / 576.0f)) + 1e-5f)));
      const float rden = __builtin_amdgcn_rcpf(den);
      v4u na, nb;
#pragma unroll
      for (int i = 0; i < 4; ++i) {
        const u32 d0 = pk_bf(v[2 * i] - m, v[2 * i + 1] - m), d1 = pk_bf(v[8 + 2 * i] - m, v[9 + 2 * i] - m);
        na[i] = pk_bf(bf_lo(d0) * rden, bf_hi(d0) * rden);
        nb[i] = pk_bf(bf_lo(d1) * rden, bf_hi(d1) * rden);
      }
      lds_st<v4u>(L_XN + lane * 16, na);
      lds_st<v4u>(L_XR + lane * 16, xa);
      if (lane < 8) { lds_st<v4u>(L_XN + 1024 + lane * 16, nb); lds_st<v4u>(L_XR + 1024 + lane * 16, xb); }
      if (cw == 0 && lane == 0) lds_st<u32>(F_TAG, tag32);
      lgkm0();
      STAMP(1);
      wait_flag4(F_WQ);
      STAMP(2);
      const int ks = s >> 2;
      const u32 a0 = L_WQ + t128_tr(lane, 0, cw), a1 = L_WQ + t128_tr(lane, 1, cw);
      v4f acc = {0.f, 0.f, 0.f, 0.f};
#pragma unroll
      for (int c = 0; c < 4; ++c) {
        const v8b A = cat8(lds_tr(a0 + c * 4096), lds_tr(a1 + c * 4096));
        const v8b B = __builtin_bit_cast(v8b, lds_ld<v4u>(L_XN + (ks * 18 + 4 * c + g16) * 16));
        acc = mfma32(A, B, acc);
      }
      {
        const int q = (lane & 15) >> 2, p = lane & 3, r = 128 + 4 * g16 + q;
        const int f = (q >> 1) | (((g16 ^ (g16 >> 1)) & 1) << 1);
        const v4s A = lds_tr(L_WQ + r * 128 + ((cw ^ f) << 5) + ((p >> 1) << 4) + ((p & 1) << 3));
        const v4s B = __builtin_bit_cast(v4s, lds_ld<v2u>(L_XN + (ks * 144 + 128 + 4 * g16) * 2));
        acc = __builtin_amdgcn_mfma_f32_16x16x16bf16_1k(A, B, acc, 0, 0, 0);
      }
      if (c16 < 2) {
        const float v0 = c16 ? acc[2] : acc[0], v1 = c16 ? acc[3] : acc[1];
        gst(rws, WS_QP + g * 8192u + (u32)(ks * 128 + (s & 3) * 32 + cw * 8 + 2 * g16 + c16) * 16u,
            (v4u){__float_as_uint(v0), tag32, __float_as_uint(v1), tag32});
      }
      lgkm0();
      set_flag(F_QD + 4 * cw, lane);
      STAMP(3);
    }
    const u32 t10 = tag32 & 0x3ffu;
    wait_flag4(F_Q);
    STAMP(4);
    {
      float q0[2] = {0.f, 0.f}, q1[2] = {0.f, 0.f};
      u32 bad = 0;
#pragma unroll
      for (int k = 0; k < 4; ++k) {
        const v4u u0 = lds_ld<v4u>(L_QST + (k * 128 + lane) * 16), u1 = lds_ld<v4u>(L_QST + (k * 128 + 64 + lane) * 16);
        q0[0] += __uint_as_float(u0.x); q0[1] += __uint_as_float(u0.z);
        q1[0] += __uint_as_float(u1.x); q1[1] += __uint_as_float(u1.z);
        bad |= (u0.y ^ tag32) | (u0.w ^ tag32) | (u1.y ^ tag32) | (u1.w ^ tag32);
      }
      if (!__all(bad == 0u)) {
#pragma unroll 1
        for (u32 it = 0; it < SPIN_MAX; ++it) {
          cbar();
          bad = 0; q0[0] = q0[1] = q1[0] = q1[1] = 0.f;
#pragma unroll
          for (int k = 0; k < 4; ++k) {
            const v4u u0 = gld(rws, WS_QP + g * 8192u + (k * 128 + lane) * 16), u1 = gld(rws, WS_QP + g * 8192u + (k * 128 + 64 + lane) * 16);
            q0[0] += __uint_as_float(u0.x); q0[1] += __uint_as_float(u0.z);
            q1[0] += __uint_as_float(u1.x); q1[1] += __uint_as_float(u1.z);
            bad |= (u0.y ^ tag32) | (u0.w ^ tag32) | (u1.y ^ tag32) | (u1.w ^ tag32);
          }
          if (__all(bad == 0u)) break;
        }
        STAMP(15);
      }
      lds_st<u32>(L_QV + cw * 576 + lane * 4, pk_bf(q0[0], q0[1]));
      lds_st<u32>(L_QV + cw * 576 + 272 + lane * 4, pk_bf(q1[0], q1[1]));
    }
    {
      const u32 key = 16 * cw + c16;
      v4f acc = {0.f, 0.f, 0.f, 0.f};
#pragma unroll
      for (int c = 0; c < 4; ++c) {
        const v8b A = __builtin_bit_cast(v8b, lds_ld<v4u>(L_K + off_b(key, 4 * c + g16)));
        const v8b B = __builtin_bit_cast(v8b, lds_ld<v4u>(L_QV + cw * 576 + (lane & 1) * 272 + (32 * c + 8 * g16) * 2));
        acc = mfma32(A, B, acc);
      }
      v4f sc;
#pragma unroll
      for (int i = 0; i < 4; ++i) sc[i] = rbf(rbf(acc[i]) * 0.08838834764831845f);
      if (c16 < 2) lds_st<v4f>(L_SC + c16 * 512 + (16 * cw + 4 * g16) * 4, sc);
      lgkm0();
      if (lane == 0) __hip_atomic_fetch_add((LAS u32*)(size_t)F_SC, 1u, __ATOMIC_RELAXED, __HIP_MEMORY_SCOPE_WORKGROUP);
      wait_cnt(F_SC, 8);
    }
    STAMP(5);
    {
      const v4f sc = lds_ld<v4f>(L_SC + (lane >> 5) * 512 + (lane & 31) * 16);
      float m = fmaxf(fmaxf(sc[0], sc[1]), fmaxf(sc[2], sc[3]));
      m = x16_max(r16_max(m));
      v4f e;
#pragma unroll
      for (int i = 0; i < 4; ++i) e[i] = __builtin_amdgcn_exp2f((sc[i] - m) * 1.4426950408889634f);
      float l = (e[0] + e[1]) + (e[2] + e[3]);
      l = x16_sum(r16_sum(l));
      lds_st<v2u>(L_P + cw * 576 + (lane >> 5) * 272 + (lane & 31) * 8, (v2u){pk_bf(e[0], e[1]), pk_bf(e[2], e[3])});
      if (cw == 0) {
        const float m1 = __uint_as_float(__builtin_amdgcn_readlane(__float_as_uint(m), 32)), l1 = __uint_as_float(__builtin_amdgcn_readlane(__float_as_uint(l), 32));
        if (lane == 0) gst(rws, WS_OP + (g * 16 + s) * OPS + 1024, pack4((v4f){m, l, m1, l1}, t10));
      }
    }
    v8b PB[4];
#pragma unroll
    for (int c = 0; c < 4; ++c) PB[c] = __builtin_bit_cast(v8b, lds_ld<v4u>(L_P + cw * 576 + (lane & 1) * 272 + (32 * c + 8 * g16) * 2));
    wait_flag4(F_V);
    STAMP(6);
    {
      const int q = (lane & 15) >> 2, p = lane & 3;
      v4f acc = {0.f, 0.f, 0.f, 0.f};
#pragma unroll
      for (int c = 0; c < 4; ++c) {
        const u32 r0 = 32 * c + 8 * g16 + q;
        const v4s lo = lds_tr(L_V + off_b(r0, 2 * cw + (p >> 1)) + 8 * (p & 1));
        const v4s hi = lds_tr(L_V + off_b(r0 + 4, 2 * cw + (p >> 1)) + 8 * (p & 1));
        acc = mfma32(cat8(lo, hi), PB[c], acc);
      }
      if (c16 < 2) gst(rws, WS_OP + (g * 16 + s) * OPS + c16 * 512 + (16 * cw + 4 * g16) * 4, pack4(acc, t10));
    }
    STAMP(7);
    if (role == 3) {
      const int r = (j - 27) * 8 + x;
      if (r >= 9 || cw != 0) { STAMP_FLUSH(); return; }
      const v4f hs = gather_rows<8>(rws, WS_HP + r * 256, lane, t10);
      const v2u xr = lds_ld<v2u>(L_XR + (64 * r + 4 * c16) * 2);
      float h[4];
      h[0] = rbf(bf_lo(xr.x) + rbf(hs[0])); h[1] = rbf(bf_hi(xr.x) + rbf(hs[1]));
      h[2] = rbf(bf_lo(xr.y) + rbf(hs[2])); h[3] = rbf(bf_hi(xr.y) + rbf(hs[3]));
      STAMP(8);
      const v4f fs = gather_rows<8>(rws, WS_FP + r * 256, lane, t10);
      STAMP(9);
      if (lane < 16) {
        const v2u o = {pk_bf(h[0] + rbf(fs[0]), h[1] + rbf(fs[1])), pk_bf(h[2] + rbf(fs[2]), h[3] + rbf(fs[3]))};
        *(v2u*)((char*)a.out + (64 * r + 4 * c16) * 2) = o;
      }
      if (r == 0 && lane == 0) __builtin_amdgcn_raw_buffer_store_b32(tag32, rws, WS_EP, 0, 16);
      STAMP(10);
      STAMP_FLUSH();
      return;
    }
  }

  const u32 t10 = tag32 & 0x3ffu;
  const int g16 = lane >> 4, c16 = lane & 15;
  wait_flag4(F_S);
  STAMP(8);
  v8b A[4][2];
  {
    const u32 sb = L_S + cw * 8192;
#pragma unroll
    for (int nb = 0; nb < 4; ++nb)
#pragma unroll
      for (int c = 0; c < 2; ++c)
        A[nb][c] = cat8(lds_tr(sb + t128_tr(lane, 0, nb) + c * 4096), lds_tr(sb + t128_tr(lane, 1, nb) + c * 4096));
  }
  u32 vec;
  if (role == 0) {
    const int gg = 2 * x + (cw >> 2), hh = (cw >> 1) & 1;
    const u32 ob = WS_OP + (gg * 16 + 4 * g16) * OPS;
    v4f o[4], st[4];
#pragma unroll 1
    for (u32 it = 0;; ++it) {
      cbar();
      u32 bad = 0;
      v4u w[4], u[4];
#pragma unroll
      for (int i = 0; i < 4; ++i) { w[i] = gld(rws, ob + i * OPS + hh * 512 + (cw & 1) * 256 + c16 * 16); u[i] = gld(rws, ob + i * OPS + 1024); }
#pragma unroll
      for (int i = 0; i < 4; ++i) { o[i] = unpack4(w[i], t10, bad); st[i] = unpack4(u[i], t10, bad); }
      if (all_ok(bad) || it >= SPIN_MAX) break;
    }
    STAMP(9);
    float m[4], l[4];
#pragma unroll
    for (int i = 0; i < 4; ++i) { m[i] = hh ? st[i][2] : st[i][0]; l[i] = hh ? st[i][3] : st[i][1]; }
    float M = fmaxf(fmaxf(m[0], m[1]), fmaxf(m[2], m[3]));
    M = x32_max(x16_max(M));
    float L = 0.f;
    v4f acc = {0.f, 0.f, 0.f, 0.f};
#pragma unroll
    for (int i = 0; i < 4; ++i) {
      const float e = __builtin_amdgcn_exp2f((m[i] - M) * 1.4426950408889634f);
      L += e * l[i];
      acc += e * o[i];
    }
    L = x32_sum(x16_sum(L));
    const float rl = __builtin_amdgcn_rcpf(L);
#pragma unroll
    for (int i = 0; i < 4; ++i) acc[i] = x32_sum(x16_sum(acc[i])) * rl;
    vec = L_VEC + cw * 128;
    if (lane < 16) lds_st<v2u>(vec + c16 * 8, (v2u){pk_bf(acc[0], acc[1]), pk_bf(acc[2], acc[3])});
  } else if (role == 1) {
    const v4f hs = gather_rows<8>(rws, WS_HP + cw * 256, lane, t10);
    STAMP(9);
    const v2u xr = lds_ld<v2u>(L_XR + (64 * cw + 4 * c16) * 2);
    const u32 h01 = pk_bf(bf_lo(xr.x) + rbf(hs[0]), bf_hi(xr.x) + rbf(hs[1]));
    const u32 h23 = pk_bf(bf_lo(xr.y) + rbf(hs[2]), bf_hi(xr.y) + rbf(hs[3]));
    const float h0 = bf_lo(h01), h1 = bf_hi(h01), h2 = bf_lo(h23), h3 = bf_hi(h23);
    const float mw = r16_sum((h0 + h1) + (h2 + h3)) * (1.0f / 64.0f);
    const float d0 = h0 - mw, d1 = h1 - mw, d2 = h2 - mw, d3 = h3 - mw;
    const float m2 = r16_sum((d0 * d0 + d1 * d1) + (d2 * d2 + d3 * d3));
    if (lane < 16) lds_st<v2u>(L_H + (64 * cw + 4 * c16) * 2, (v2u){h01, h23});
    if (lane == 0) lds_st<v2f>(L_ST + cw * 8, (v2f){mw, m2});
    wg_barrier();
    const v2f sw = lds_ld<v2f>(L_ST + (c16 < 9 ? c16 : 0) * 8);
    const u32 hv = lds_ld<unsigned short>(L_H + (64 * jj + lane) * 2);
    const float mean = r16_sum(c16 < 9 ? sw[0] : 0.f) * (1.0f / 9.0f);
    const float dm = sw[0] - mean;
    const float var = r16_sum(c16 < 9 ? sw[1] + 64.0f * dm * dm : 0.f) * (1.0f / 576.0f);
    const float den = rbf(__builtin_amdgcn_sqrtf(rbf(rbf(var) + 1e-5f)));
    const float xn = rbf(bf_lo(hv) - rbf(mean)) * __builtin_amdgcn_rcpf(den);
    vec = L_XN2 + cw * 128;
    lds_st<unsigned short>(vec + lane * 2, (unsigned short)pk_bf(xn, xn));
  } else {
    const v4f ps = gather_rows<9>(rws, WS_PP + x * 9 * 2304 + cw * 256, lane, t10);
    STAMP(9);
    float gv[4];
#pragma unroll
    for (int i = 0; i < 4; ++i) {
      const float pre = rbf(ps[i]);
      gv[i] = pre * __builtin_amdgcn_rcpf(1.0f + __builtin_amdgcn_exp2f(pre * -1.4426950408889634f));
    }
    vec = L_VEC + cw * 128;
    if (lane < 16) lds_st<v2u>(vec + c16 * 8, (v2u){pk_bf(gv[0], gv[1]), pk_bf(gv[2], gv[3])});
  }
  v4f r;
  {
    const v8b B0 = __builtin_bit_cast(v8b, lds_ld<v4u>(vec + g16 * 16)), B1 = __builtin_bit_cast(v8b, lds_ld<v4u>(vec + 64 + g16 * 16));
    const v4f z = {0.f, 0.f, 0.f, 0.f};
    const v4f c0 = mfma32(A[0][1], B1, mfma32(A[0][0], B0, z)), c1 = mfma32(A[1][1], B1, mfma32(A[1][0], B0, z));
    const v4f c2 = mfma32(A[2][1], B1, mfma32(A[2][0], B0, z)), c3 = mfma32(A[3][1], B1, mfma32(A[3][0], B0, z));
    r = pick4(c0, c1, c2, c3, c16);
  }
  if (role == 1) {
    if (c16 < 4) gst(rws, WS_PP + (x * 9 + jj) * 2304 + cw * 256 + (16 * c16 + 4 * g16) * 4, pack4(r, t10));
    STAMP(10);
    STAMP_FLUSH();
    return;
  }
  if (c16 < 4) lds_st<v4f>(L_PART + cw * 256 + (16 * c16 + 4 * g16) * 4, r);
  wg_barrier();
  if (cw == 0) {
    v4f t = lds_ld<v4f>(L_PART + g16 * 256 + c16 * 16) + lds_ld<v4f>(L_PART + (g16 + 4) * 256 + c16 * 16);
    if (role == 2) { const v4f e = lds_ld<v4f>(L_PART + 8 * 256 + c16 * 16); if (g16 == 0) t += e; }
#pragma unroll
    for (int i = 0; i < 4; ++i) t[i] = x32_sum(x16_sum(t[i]));
    if (lane < 16) gst(rws, (role == 0 ? WS_HP : WS_FP) + x * 2304 + jj * 256 + c16 * 16, pack4(t, t10));
  }
  STAMP(10);
  STAMP_FLUSH();
}

}

extern "C" void kernel_launch(void* const* d_in, const int* in_sizes, int n_in, void* d_out, int out_size, void* d_ws, size_t ws_size,
                              hipStream_t stream) {
  (void)in_sizes; (void)n_in; (void)out_size; (void)ws_size;
  Args a;
  a.x = d_in[0]; a.kc = d_in[1]; a.vc = d_in[2]; a.wq = d_in[3]; a.wo = d_in[4]; a.w1 = d_in[6]; a.w2 = d_in[7];
  a.out = d_out; a.ws = d_ws;
  (void)hipFuncSetAttribute((const void*)fused_decode_kernel, hipFuncAttributeMaxDynamicSharedMemorySize, (int)L_END);
  hipLaunchKernelGGL(fused_decode_kernel, dim3(256), dim3(768), L_END, stream, a);
}
```

```cpp
#include <hip/hip_runtime.h>
#include <hip/hip_bf16.h>
#include <stdint.h>

constexpr int D = 576, HQ = 32, HKV = 16, SK = 8192, DH = 128, W = 2048, F = 4608;

__device__ __forceinline__ float bf2f(unsigned short b) { return __uint_as_float(((unsigned)b) << 16); }
__device__ __forceinline__ unsigned short f2bf(float f) { unsigned u = __float_as_uint(f); u += 0x7FFFu + ((u >> 16) & 1u); return (unsigned short)(u >> 16); }
__device__ __forceinline__ float rbf(float f) { return bf2f(f2bf(f)); }

#define MIMIC 1
__device__ __forceinline__ float R(float f) { return MIMIC ? rbf(f) : f; }

__device__ void layernorm_block(const float* in, float* out, float* red) {
  int tid = threadIdx.x;
  if (tid == 0) {
    float s = 0; for (int i = 0; i < D; i++) s += in[i];
    float m = R(s / D);
    float mean = s / D;
    float v = 0; for (int i = 0; i < D; i++) { float d = in[i] - mean; v += d * d; }
    v = R(v / D);
    float den = R(sqrtf(R(v + 1e-5f)));
    red[0] = m; red[1] = den;
  }
  __syncthreads();
  for (int i = tid; i < D; i += blockDim.x) out[i] = R(R(in[i] - red[0]) / red[1]);
  __syncthreads();
}

__global__ void k_q(const unsigned short* x, const unsigned short* wq, float* q) {
  __shared__ float xs[D], xn[D], red[2];
  for (int i = threadIdx.x; i < D; i += blockDim.x) xs[i] = bf2f(x[i]);
  __syncthreads();
  layernorm_block(xs, xn, red);
  int col = blockIdx.x * blockDim.x + threadIdx.x;
  float acc = 0;
  for (int i = 0; i < D; i++) acc += xn[i] * bf2f(wq[(size_t)i * 4096 + col]);
  q[col] = R(acc);
}

__global__ void k_attn(const float* q, const unsigned short* kc, const unsigned short* vc, float* o) {
  __shared__ float sc[W]; __shared__ float qs[DH]; __shared__ float red[256];
  int h = blockIdx.x, g = h / 2, tid = threadIdx.x;
  if (tid < DH) qs[tid] = q[h * DH + tid];
  __syncthreads();
  const unsigned short* K = kc + ((size_t)g * SK + (SK - W)) * DH;
  const unsigned short* V = vc + ((size_t)g * SK + (SK - W)) * DH;
  float mx = -INFINITY;
  for (int j = tid; j < W; j += 256) {
    float a = 0; for (int d = 0; d < DH; d++) a += qs[d] * bf2f(K[(size_t)j * DH + d]);
    a = R(R(a) * 0.08838834764831845f);
    sc[j] = a; mx = fmaxf(mx, a);
  }
  red[tid] = mx; __syncthreads();
  for (int s = 128; s > 0; s >>= 1) { if (tid < s) red[tid] = fmaxf(red[tid], red[tid + s]); __syncthreads(); }
  mx = red[0]; __syncthreads();
  float sum = 0;
  for (int j = tid; j < W; j += 256) { float e = expf(sc[j] - mx); sc[j] = e; sum += e; }
  red[tid] = sum; __syncthreads();
  for (int s = 128; s > 0; s >>= 1) { if (tid < s) red[tid] += red[tid + s]; __syncthreads(); }
  sum = red[0]; __syncthreads();
  for (int j = tid; j < W; j += 256) sc[j] = R(sc[j] / sum);
  __syncthreads();
  if (tid < DH) {
    float a = 0; for (int j = 0; j < W; j++) a += sc[j] * bf2f(V[(size_t)j * DH + tid]);
    o[h * DH + tid] = R(a);
  }
}

__global__ void k_wo(const float* o, const unsigned short* wo, const unsigned short* x, float* h) {
  int col = blockIdx.x * blockDim.x + threadIdx.x;
  if (col >= D) return;
  float acc = 0;
  for (int i = 0; i < 4096; i++) acc += o[i] * bf2f(wo[(size_t)i * D + col]);
  h[col] = R(bf2f(x[col]) + R(acc));
}

__global__ void k_w1(const float* h, const unsigned short* w1, float* g) {
  __shared__ float xs[D], xn[D], red[2];
  for (int i = threadIdx.x; i < D; i += blockDim.x) xs[i] = h[i];
  __syncthreads();
  layernorm_block(xs, xn, red);
  int col = blockIdx.x * blockDim.x + threadIdx.x;
  float acc = 0;
  for (int i = 0; i < D; i++) acc += xn[i] * bf2f(w1[(size_t)i * F + col]);
  acc = R(acc);
  g[col] = R(acc / (1.0f + expf(-acc)));
}

__global__ void k_w2(const float* g, const unsigned short* w2, const float* h, unsigned short* out) {
  int col = blockIdx.x * blockDim.x + threadIdx.x;
  if (col >= D) return;
  float acc = 0;
  for (int i = 0; i < F; i++) acc += g[i] * bf2f(w2[(size_t)i * D + col]);
  out[col] = f2bf(h[col] + R(acc));
}

extern "C" void kernel_launch(void* const* d_in, const int* in_sizes, int n_in,
                              void* d_out, int out_size, void* d_ws, size_t ws_size,
                              hipStream_t stream) {
  const unsigned short* x = (const unsigned short*)d_in[0];
  const unsigned short* kc = (const unsigned short*)d_in[1];
  const unsigned short* vc = (const unsigned short*)d_in[2];
  const unsigned short* wq = (const unsigned short*)d_in[3];
  const unsigned short* wo = (const unsigned short*)d_in[4];
  const unsigned short* w1 = (const unsigned short*)d_in[6];
  const unsigned short* w2 = (const unsigned short*)d_in[7];
  float* ws = (float*)d_ws;
  float* q = ws; float* o = ws + 4096; float* h = ws + 8192; float* g = ws + 9216;
  k_q<<<16, 256, 0, stream>>>(x, wq, q);
  k_attn<<<32, 256, 0, stream>>>(q, kc, vc, o);
  k_wo<<<9, 64, 0, stream>>>(o, wo, x, h);
  k_w1<<<18, 256, 0, stream>>>(h, w1, g);
  k_w2<<<9, 64, 0, stream>>>(g, w2, h, (unsigned short*)d_out);
}
```

```cpp
#define AMP 0
#include <hip/hip_runtime.h>
#include <stdint.h>
#include <type_traits>

namespace {

#define LAS __attribute__((address_space(3)))
typedef unsigned int u32;
typedef u32 v4u __attribute__((ext_vector_type(4)));
typedef u32 v2u __attribute__((ext_vector_type(2)));
typedef float v4f __attribute__((ext_vector_type(4)));
typedef float v2f __attribute__((ext_vector_type(2)));
typedef short v4s __attribute__((ext_vector_type(4)));
typedef short v8s __attribute__((ext_vector_type(8)));
typedef __bf16 v8b __attribute__((ext_vector_type(8)));
typedef __bf16 v2b __attribute__((ext_vector_type(2)));
typedef __amdgpu_buffer_rsrc_t rsrc_t;

#define WIN 10
#ifndef NV1
#define NV1 4
#endif
#define BULK_AUX 1

constexpr u32 L_WQ = 0, L_K = 18432, L_V = 51200, L_S = 83968, L_FL = 157696;
constexpr u32 L_XN = L_FL + 128, L_SC = L_XN + 1152, L_Y = L_SC + 1024, L_END = 163840;
constexpr u32 L_QST = 0, L_QV = 8192, L_P = L_QV + 8 * 576;
constexpr u32 F_WQ = L_FL, F_Q = L_FL + 16, F_V = L_FL + 32, F_S = L_FL + 48, F_QD = L_FL + 64, F_SC = L_FL + 80, F_TAG = L_FL + 96;
constexpr u32 L_XR = L_Y, L_ST = L_Y + 1152, L_H = L_ST + 128, L_XN2 = L_H + 1152;
constexpr u32 L_VEC = L_Y, L_PART = L_Y + 1152;
static_assert(L_XN2 + 9 * 128 <= L_END && L_PART + 9 * 256 <= L_END, "lds");
constexpr u32 WS_EP = 0, WS_QP = 4096, WS_OP = WS_QP + 16 * 8192, OPS = 1152, WS_HP = WS_OP + 256 * OPS;
constexpr u32 WS_PP = WS_HP + 8 * 2304, WS_FP = WS_PP + 72 * 2304, WS_END = WS_FP + 8 * 2304;
constexpr u32 SPIN_MAX = 1u << 16;

struct Args {
  const void* x; const void* kc; const void* vc; const void* wq; const void* wo; const void* w1; const void* w2;
  void* out; void* ws;
};

template <int N, class F> __device__ __forceinline__ void static_for(F&& f) {
  if constexpr (N > 0) { static_for<N - 1>(f); f(std::integral_constant<int, N - 1>{}); }
}

__device__ __forceinline__ float bf_lo(u32 u) { return __uint_as_float(u << 16); }
__device__ __forceinline__ float bf_hi(u32 u) { return __uint_as_float(u & 0xffff0000u); }
__device__ __forceinline__ u32 pk_bf(float lo, float hi) {
  v2b r = __builtin_convertvector((v2f){lo, hi}, v2b);
  return __builtin_bit_cast(u32, r);
}
__device__ __forceinline__ float rbf(float f) { return bf_lo(pk_bf(f, f)); }

template <int C> __device__ __forceinline__ float dppf(float v) {
  return __builtin_bit_cast(float, __builtin_amdgcn_update_dpp(0, __builtin_bit_cast(int, v), C, 0xf, 0xf, true));
}
__device__ __forceinline__ float r16_sum(float v) { v += dppf<0xB1>(v); v += dppf<0x4E>(v); v += dppf<0x141>(v); v += dppf<0x140>(v); return v; }
__device__ __forceinline__ float r16_max(float v) { v = fmaxf(v, dppf<0xB1>(v)); v = fmaxf(v, dppf<0x4E>(v)); v = fmaxf(v, dppf<0x141>(v)); v = fmaxf(v, dppf<0x140>(v)); return v; }
__device__ __forceinline__ float x16_sum(float v) { auto r = __builtin_amdgcn_permlane16_swap(__float_as_uint(v), __float_as_uint(v), false, false); return __uint_as_float(r[0]) + __uint_as_float(r[1]); }
__device__ __forceinline__ float x32_sum(float v) { auto r = __builtin_amdgcn_permlane32_swap(__float_as_uint(v), __float_as_uint(v), false, false); return __uint_as_float(r[0]) + __uint_as_float(r[1]); }
__device__ __forceinline__ float x16_max(float v) { auto r = __builtin_amdgcn_permlane16_swap(__float_as_uint(v), __float_as_uint(v), false, false); return fmaxf(__uint_as_float(r[0]), __uint_as_float(r[1])); }
__device__ __forceinline__ float x32_max(float v) { auto r = __builtin_amdgcn_permlane32_swap(__float_as_uint(v), __float_as_uint(v), false, false); return fmaxf(__uint_as_float(r[0]), __uint_as_float(r[1])); }

template <class T> __device__ __forceinline__ T lds_ld(u32 a) { return *(const LAS T*)(size_t)a; }
template <class T> __device__ __forceinline__ void lds_st(u32 a, T v) { *(LAS T*)(size_t)a = v; }
__device__ __forceinline__ v4s lds_tr(u32 a) { return __builtin_amdgcn_ds_read_tr16_b64_v4i16((LAS v4s*)(size_t)a); }
__device__ __forceinline__ v8b cat8(v4s a, v4s b) { return __builtin_bit_cast(v8b, __builtin_shufflevector(a, b, 0, 1, 2, 3, 4, 5, 6, 7)); }
__device__ __forceinline__ v4f mfma32(v8b a, v8b b, v4f c) { return __builtin_amdgcn_mfma_f32_16x16x32_bf16(a, b, c, 0, 0, 0); }

__device__ __forceinline__ void lgkm0() { asm volatile("s_waitcnt lgkmcnt(0)" ::: "memory"); }
template <int N> __device__ __forceinline__ void vmwait() { asm volatile("s_waitcnt vmcnt(%0)" ::"n"(N) : "memory"); }
__device__ __forceinline__ void cbar() { asm volatile("" ::: "memory"); }
__device__ __forceinline__ void wg_barrier() { lgkm0(); __builtin_amdgcn_s_barrier(); cbar(); }

template <int AUX> __device__ __forceinline__ void dma(rsrc_t r, u32 voff, u32 soff, u32 lds) {
  if constexpr (AUX == 1) asm volatile("s_mov_b32 m0, %0\n\ts_nop 0\n\tbuffer_load_dwordx4 %1, %2, %3 offen nt lds" ::"s"(lds), "v"(voff), "s"(r), "s"(soff) : "memory");
  else if constexpr (AUX == 16) asm volatile("s_mov_b32 m0, %0\n\ts_nop 0\n\tbuffer_load_dwordx4 %1, %2, %3 offen sc1 lds" ::"s"(lds), "v"(voff), "s"(r), "s"(soff) : "memory");
  else asm volatile("s_mov_b32 m0, %0\n\ts_nop 0\n\tbuffer_load_dwordx4 %1, %2, %3 offen lds" ::"s"(lds), "v"(voff), "s"(r), "s"(soff) : "memory");
}

__device__ __forceinline__ void set_flag(u32 a, int lane) {
  if (lane == 0) *(volatile LAS u32*)(size_t)a = 1u;
}
__device__ __forceinline__ void wait_flag4(u32 a) {
#pragma unroll 1
  for (u32 it = 0; it < (1u << 22); ++it) {
    v4u v;
    asm volatile("ds_read_b128 %0, %1\n\ts_waitcnt lgkmcnt(0)" : "=v"(v) : "v"(a) : "memory");
    if (__builtin_amdgcn_readfirstlane(v.x & v.y & v.z & v.w)) break;
    __builtin_amdgcn_s_sleep(1);
  }
}
__device__ __forceinline__ void wait_cnt(u32 a, u32 n) {
#pragma unroll 1
  for (u32 it = 0; it < (1u << 22); ++it) {
    u32 v;
    asm volatile("ds_read_b32 %0, %1\n\ts_waitcnt lgkmcnt(0)" : "=v"(v) : "v"(a) : "memory");
    if (__builtin_amdgcn_readfirstlane(v) >= n) break;
    __builtin_amdgcn_s_sleep(1);
  }
}

__device__ __forceinline__ v4u gld(rsrc_t r, u32 off) { return __builtin_amdgcn_raw_buffer_load_b128(r, off, 0, 16); }
__device__ __forceinline__ void gst(rsrc_t r, u32 off, v4u v) { __builtin_amdgcn_raw_buffer_store_b128(v, r, off, 0, 16); }
__device__ __forceinline__ v4u pack4(v4f v, u32 t) {
  v4u r;
#pragma unroll
  for (int i = 0; i < 4; ++i) r[i] = ((__float_as_uint(v[i]) + 0x200u) & ~0x3ffu) | t;
  return r;
}
__device__ __forceinline__ v4f unpack4(v4u w, u32 t, u32& bad) {
  v4u y = w ^ t;
  bad |= y.x | y.y | y.z | y.w;
  return __builtin_bit_cast(v4f, y);
}
__device__ __forceinline__ bool all_ok(u32 bad) { return __all((bad & 0x3ffu) == 0u); }

#define STAMP(i) do { if (i == 0) st0 = (u32)__builtin_amdgcn_s_memrealtime(); if (i == AMP) st1 = (u32)__builtin_amdgcn_s_memrealtime(); } while (0)
#define STAMP_DECL u32 st0 = 0, st1 = 0
#define STAMP_FLUSH() do { } while (0)
#define AMP_SPIN() do { const u32 d_ = (st1 - st0) * 10u; const u32 n_ = (u32)__builtin_amdgcn_s_memrealtime(); \
  for (u32 it_ = 0; it_ < (1u << 20); ++it_) { if ((u32)__builtin_amdgcn_s_memrealtime() - n_ >= d_ || d_ > 100000u) break; __builtin_amdgcn_s_sleep(2); } } while (0)

__device__ __forceinline__ u32 t128_tr(int lane, int t, int nb) {
  const int g = lane >> 4, q = (lane & 15) >> 2, p = lane & 3;
  const int r = 8 * g + 4 * t + q;
  const int f = (q >> 1) | (((t ^ g) & 1) << 1);
  return (u32)(r * 128 + ((nb ^ f) << 5) + ((p >> 1) << 4) + ((p & 1) << 3));
}
__device__ __forceinline__ u32 off_b(u32 row, u32 ch) { return 256u * row + 16u * (ch ^ (((row & 3) << 2) | ((row >> 2) & 3))); }

template <int NP> __device__ __forceinline__ v4f gather_rows(rsrc_t rws, u32 base, int lane, u32 t) {
  const int pq = lane >> 4, q = lane & 15;
  const u32 o = base + pq * 2304 + q * 16;
  v4f s;
#pragma unroll 1
  for (u32 it = 0;; ++it) {
    cbar();
    u32 bad = 0;
    v4u w0 = gld(rws, o), w1 = gld(rws, o + 4 * 2304), w2;
    if constexpr (NP == 9) w2 = gld(rws, base + 8 * 2304 + q * 16);
    s = unpack4(w0, t, bad) + unpack4(w1, t, bad);
    if constexpr (NP == 9) { v4f e = unpack4(w2, t, bad); if (pq == 0) s += e; }
    if (all_ok(bad) || it >= SPIN_MAX) break;
  }
#pragma unroll
  for (int i = 0; i < 4; ++i) s[i] = x32_sum(x16_sum(s[i]));
  return s;
}

__device__ __forceinline__ v4f pick4(v4f a0, v4f a1, v4f a2, v4f a3, int c16) {
  const bool b0 = c16 & 1, b1 = c16 & 2;
  v4f r;
#pragma unroll
  for (int i = 0; i < 4; ++i) {
    const float lo = b0 ? a1[i] : a0[i], hi = b0 ? a3[i] : a2[i];
    r[i] = b1 ? hi : lo;
  }
  return r;
}

__global__ void __launch_bounds__(768, 3) fused_decode_kernel(Args a) {
  extern __shared__ __attribute__((aligned(16))) unsigned char smem[];
  const int tid = threadIdx.x, lane = tid & 63;
  const int wv = __builtin_amdgcn_readfirstlane(tid >> 6);
  const int b = blockIdx.x, x = b & 7, j = b >> 3;
  const int g = 2 * x + (j >> 4), s = j & 15;
  const int role = j < 9 ? 0 : (j < 18 ? 1 : (j < 27 ? 2 : 3));
  const int jj = j - 9 * role;
  const rsrc_t rws = __builtin_amdgcn_make_buffer_rsrc(a.ws, 0, WS_END, 0x00020000);
  STAMP_DECL;
  STAMP(0);
  int cw;
  u32 tag32;

  if (wv < 4) {
    const int lw = wv;
    constexpr int W = WIN;
    constexpr int N_WQ = 5, N_K = 13, N_V1 = 13 + NV1, N_QG = N_V1 + 2, N_V2 = 23;
    auto after = [&](auto n_) {
      constexpr int n = decltype(n_)::value;
      if constexpr (n > W) vmwait<W>();
      if constexpr (n - W == N_WQ) set_flag(F_WQ + 4 * lw, lane);
      if constexpr (n - W == N_QG) set_flag(F_Q + 4 * lw, lane);
      if constexpr (n - W == N_V2) set_flag(F_V + 4 * lw, lane);
    };
    const u32 vt = (u32)(((lane & 7) ^ (((lane >> 4) & 1) << 1) ^ (((lane >> 5) & 1) << 2)) * 16);
    {
      const rsrc_t rq = __builtin_amdgcn_make_buffer_rsrc((void*)a.wq, 0, 576 * 4096 * 2, 0x00020000);
      const u32 voff = (u32)(lane >> 3) * 8192u + vt;
      const u32 base = (u32)(s >> 2) * 144u * 8192u + (u32)(g * 256 + (s & 3) * 64) * 2u;
      static_for<5>([&](auto k_) {
        constexpr int k = decltype(k_)::value;
        const int i = min(lw + 4 * k, 17);
        dma<BULK_AUX>(rq, voff ^ (u32)((i & 1) << 6), base + i * 65536u, L_WQ + i * 1024u);
        after(std::integral_constant<int, k + 1>{});
      });
    }
    __builtin_amdgcn_s_barrier();
    cbar();
    const u32 vkv = ((u32)(lane >> 4) * 256u + (u32)(((lane & 15) ^ ((lane >> 4) << 2)) * 16)) ^ (u32)(lw << 4);
    const u32 bkv = (u32)(g * 8192 + 6144 + s * 128) * 256u + lw * 1024u;
    const rsrc_t rk = __builtin_amdgcn_make_buffer_rsrc((void*)a.kc, 0, 16 * 8192 * 256, 0x00020000);
    const rsrc_t rv = __builtin_amdgcn_make_buffer_rsrc((void*)a.vc, 0, 16 * 8192 * 256, 0x00020000);
    static_for<8>([&](auto k_) {
      constexpr int k = decltype(k_)::value;
      dma<BULK_AUX>(rk, vkv, bkv + k * 4096u, L_K + lw * 1024u + k * 4096u);
      after(std::integral_constant<int, N_WQ + k + 1>{});
    });
    static_for<NV1>([&](auto k_) {
      constexpr int k = decltype(k_)::value;
      dma<BULK_AUX>(rv, vkv, bkv + k * 4096u, L_V + lw * 1024u + k * 4096u);
      after(std::integral_constant<int, N_K + k + 1>{});
    });
    vmwait<NV1>();
    if constexpr (N_V1 - W < N_WQ) set_flag(F_WQ + 4 * lw, lane);
    STAMP(1);
    wait_flag4(F_QD);
    static_for<2>([&](auto k_) {
      constexpr int k = decltype(k_)::value;
      dma<16>(rws, (u32)lane * 16u, WS_QP + g * 8192u + (lw + 4 * k) * 1024u, L_QST + (lw + 4 * k) * 1024u);
      after(std::integral_constant<int, N_V1 + k + 1>{});
    });
    static_for<8 - NV1>([&](auto k_) {
      constexpr int k = decltype(k_)::value;
      dma<BULK_AUX>(rv, vkv, bkv + (NV1 + k) * 4096u, L_V + lw * 1024u + (NV1 + k) * 4096u);
      after(std::integral_constant<int, N_QG + k + 1>{});
    });
    if (role < 3) {
      const void* wp = role == 0 ? a.wo : (role == 1 ? a.w1 : a.w2);
      const u32 wbytes = role == 0 ? 4096u * 576u * 2u : 4608u * 576u * 2u;
      const rsrc_t rw = __builtin_amdgcn_make_buffer_rsrc((void*)wp, 0, wbytes, 0x00020000);
      const u32 rs = role == 1 ? 9216u : 1152u;
      const u32 base = role == 0 ? (u32)x * 512u * 1152u + (u32)jj * 128u
                     : role == 1 ? (u32)jj * 64u * 9216u + (u32)x * 1152u
                                 : (u32)x * 576u * 1152u + (u32)jj * 128u;
      const int nk = role == 0 ? 16 : 18;
      const u32 voff = (u32)(lane >> 3) * rs + vt;
      static_for<18>([&](auto k_) {
        constexpr int k = decltype(k_)::value;
        if (k < nk) {
          const u32 i = lw + 4 * k;
          const u32 rb = role == 1 ? (i * 7282u) >> 16 : i;
          const u32 p = role == 1 ? i - rb * 9u : 0u;
          dma<BULK_AUX>(rw, voff ^ ((rb & 1u) << 6), base + rb * 8u * rs + p * 128u, L_S + (p * 8u + rb) * 1024u);
          after(std::integral_constant<int, N_V2 + k + 1>{});
        }
      });
      static_assert(N_V2 + W <= N_V2 + 16, "flags must be inside the guarded range");
    } else {
      if constexpr (N_V2 - W < N_QG) { vmwait<8 - NV1>(); set_flag(F_Q + 4 * lw, lane); }
      vmwait<0>();
      set_flag(F_V + 4 * lw, lane);
    }
    vmwait<0>();
    set_flag(F_S + 4 * lw, lane);
    STAMP(2);
    if (lw != 0 || role == 0 || role == 3) { STAMP_FLUSH(); return; }
    lgkm0();
    tag32 = lds_ld<u32>(F_TAG);
    cw = 8;
  } else {
    cw = wv - 4;
    const u32 E = __builtin_amdgcn_raw_buffer_load_b32(rws, WS_EP, 0, 16);
    v4u xa, xb;
    if (cw < 4) {
      const rsrc_t rx = __builtin_amdgcn_make_buffer_rsrc((void*)a.x, 0, 1152, 0x00020000);
      xa = __builtin_amdgcn_raw_buffer_load_b128(rx, lane * 16, 0, 0);
      xb = __builtin_amdgcn_raw_buffer_load_b128(rx, 1024 + lane * 16, 0, 0);
    }
    if (cw == 7 && lane < 32) lds_st<u32>(L_FL + lane * 4, 0u);
    wg_barrier();
    tag32 = __builtin_amdgcn_readfirstlane(E) + 1u;
    const int g16 = lane >> 4, c16 = lane & 15;
    if (cw < 4) {
      float v[16];
#pragma unroll
      for (int i = 0; i < 4; ++i) { v[2 * i] = bf_lo(xa[i]); v[2 * i + 1] = bf_hi(xa[i]); v[8 + 2 * i] = bf_lo(xb[i]); v[9 + 2 * i] = bf_hi(xb[i]); }
      float sm = 0.f;
#pragma unroll
      for (int i = 0; i < 16; ++i) sm += v[i];
      sm = x32_sum(x16_sum(r16_sum(sm)));
      const float mean = sm * (1.0f / 576.0f);
      float s1 = 0.f, s2 = 0.f;
#pragma unroll
      for (int i = 0; i < 8; ++i) { const float d = v[i] - mean, e = v[8 + i] - mean; s1 += d * d; s2 += e * e; }
      if (lane < 8) s1 += s2;
      s1 = x32_sum(x16_sum(r16_sum(s1)));
      const float m = rbf(mean);
      const float den = rbf(__builtin_amdgcn_sqrtf(rbf(rbf(s1 * (1.0f / 576.0f)) + 1e-5f)));
      const float rden = __builtin_amdgcn_rcpf(den);
      v4u na, nb;
#pragma unroll
      for (int i = 0; i < 4; ++i) {
        const u32 d0 = pk_bf(v[2 * i] - m, v[2 * i + 1] - m), d1 = pk_bf(v[8 + 2 * i] - m, v[9 + 2 * i] - m);
        na[i] = pk_bf(bf_lo(d0) * rden, bf_hi(d0) * rden);
        nb[i] = pk_bf(bf_lo(d1) * rden, bf_hi(d1) * rden);
      }
      lds_st<v4u>(L_XN + lane * 16, na);
      lds_st<v4u>(L_XR + lane * 16, xa);
      if (lane < 8) { lds_st<v4u>(L_XN + 1024 + lane * 16, nb); lds_st<v4u>(L_XR + 1024 + lane * 16, xb); }
      if (cw == 0 && lane == 0) lds_st<u32>(F_TAG, tag32);
      lgkm0();
      STAMP(1);
      wait_flag4(F_WQ);
      STAMP(2);
      const int ks = s >> 2;
      const u32 a0 = L_WQ + t128_tr(lane, 0, cw), a1 = L_WQ + t128_tr(lane, 1, cw);
      v4f acc = {0.f, 0.f, 0.f, 0.f};
#pragma unroll
      for (int c = 0; c < 4; ++c) {
        const v8b A = cat8(lds_tr(a0 + c * 4096), lds_tr(a1 + c * 4096));
        const v8b B = __builtin_bit_cast(v8b, lds_ld<v4u>(L_XN + (ks * 18 + 4 * c + g16) * 16));
        acc = mfma32(A, B, acc);
      }
      {
        const int q = (lane & 15) >> 2, p = lane & 3, r = 128 + 4 * g16 + q;
        const int f = (q >> 1) | (((g16 ^ (g16 >> 1)) & 1) << 1);
        const v4s A = lds_tr(L_WQ + r * 128 + ((cw ^ f) << 5) + ((p >> 1) << 4) + ((p & 1) << 3));
        const v4s B = __builtin_bit_cast(v4s, lds_ld<v2u>(L_XN + (ks * 144 + 128 + 4 * g16) * 2));
        acc = __builtin_amdgcn_mfma_f32_16x16x16bf16_1k(A, B, acc, 0, 0, 0);
      }
      if (c16 < 2) {
        const float v0 = c16 ? acc[2] : acc[0], v1 = c16 ? acc[3] : acc[1];
        gst(rws, WS_QP + g * 8192u + (u32)(ks * 128 + (s & 3) * 32 + cw * 8 + 2 * g16 + c16) * 16u,
            (v4u){__float_as_uint(v0), tag32, __float_as_uint(v1), tag32});
      }
      lgkm0();
      set_flag(F_QD + 4 * cw, lane);
      STAMP(3);
    }
    const u32 t10 = tag32 & 0x3ffu;
    wait_flag4(F_Q);
    STAMP(4);
    {
      float q0[2] = {0.f, 0.f}, q1[2] = {0.f, 0.f};
      u32 bad = 0;
#pragma unroll
      for (int k = 0; k < 4; ++k) {
        const v4u u0 = lds_ld<v4u>(L_QST + (k * 128 + lane) * 16), u1 = lds_ld<v4u>(L_QST + (k * 128 + 64 + lane) * 16);
        q0[0] += __uint_as_float(u0.x); q0[1] += __uint_as_float(u0.z);
        q1[0] += __uint_as_float(u1.x); q1[1] += __uint_as_float(u1.z);
        bad |= (u0.y ^ tag32) | (u0.w ^ tag32) | (u1.y ^ tag32) | (u1.w ^ tag32);
      }
      if (!__all(bad == 0u)) {
#pragma unroll 1
        for (u32 it = 0; it < SPIN_MAX; ++it) {
          cbar();
          bad = 0; q0[0] = q0[1] = q1[0] = q1[1] = 0.f;
#pragma unroll
          for (int k = 0; k < 4; ++k) {
            const v4u u0 = gld(rws, WS_QP + g * 8192u + (k * 128 + lane) * 16), u1 = gld(rws, WS_QP + g * 8192u + (k * 128 + 64 + lane) * 16);
            q0[0] += __uint_as_float(u0.x); q0[1] += __uint_as_float(u0.z);
            q1[0] += __uint_as_float(u1.x); q1[1] += __uint_as_float(u1.z);
            bad |= (u0.y ^ tag32) | (u0.w ^ tag32) | (u1.y ^ tag32) | (u1.w ^ tag32);
          }
          if (__all(bad == 0u)) break;
        }
        STAMP(15);
      }
      lds_st<u32>(L_QV + cw * 576 + lane * 4, pk_bf(q0[0], q0[1]));
      lds_st<u32>(L_QV + cw * 576 + 272 + lane * 4, pk_bf(q1[0], q1[1]));
    }
    {
      const u32 key = 16 * cw + c16;
      v4f acc = {0.f, 0.f, 0.f, 0.f};
#pragma unroll
      for (int c = 0; c < 4; ++c) {
        const v8b A = __builtin_bit_cast(v8b, lds_ld<v4u>(L_K + off_b(key, 4 * c + g16)));
        const v8b B = __builtin_bit_cast(v8b, lds_ld<v4u>(L_QV + cw * 576 + (lane & 1) * 272 + (32 * c + 8 * g16) * 2));
        acc = mfma32(A, B, acc);
      }
      v4f sc;
#pragma unroll
      for (int i = 0; i < 4; ++i) sc[i] = rbf(rbf(acc[i]) * 0.08838834764831845f);
      if (c16 < 2) lds_st<v4f>(L_SC + c16 * 512 + (16 * cw + 4 * g16) * 4, sc);
      lgkm0();
      if (lane == 0) __hip_atomic_fetch_add((LAS u32*)(size_t)F_SC, 1u, __ATOMIC_RELAXED, __HIP_MEMORY_SCOPE_WORKGROUP);
      wait_cnt(F_SC, 8);
    }
    STAMP(5);
    {
      const v4f sc = lds_ld<v4f>(L_SC + (lane >> 5) * 512 + (lane & 31) * 16);
      float m = fmaxf(fmaxf(sc[0], sc[1]), fmaxf(sc[2], sc[3]));
      m = x16_max(r16_max(m));
      v4f e;
#pragma unroll
      for (int i = 0; i < 4; ++i) e[i] = __builtin_amdgcn_exp2f((sc[i] - m) * 1.4426950408889634f);
      float l = (e[0] + e[1]) + (e[2] + e[3]);
      l = x16_sum(r16_sum(l));
      lds_st<v2u>(L_P + cw * 576 + (lane >> 5) * 272 + (lane & 31) * 8, (v2u){pk_bf(e[0], e[1]), pk_bf(e[2], e[3])});
      if (cw == 0) {
        const float m1 = __uint_as_float(__builtin_amdgcn_readlane(__float_as_uint(m), 32)), l1 = __uint_as_float(__builtin_amdgcn_readlane(__float_as_uint(l), 32));
        if (lane == 0) gst(rws, WS_OP + (g * 16 + s) * OPS + 1024, pack4((v4f){m, l, m1, l1}, t10));
      }
    }
    v8b PB[4];
#pragma unroll
    for (int c = 0; c < 4; ++c) PB[c] = __builtin_bit_cast(v8b, lds_ld<v4u>(L_P + cw * 576 + (lane & 1) * 272 + (32 * c + 8 * g16) * 2));
    wait_flag4(F_V);
    STAMP(6);
    {
      const int q = (lane & 15) >> 2, p = lane & 3;
      v4f acc = {0.f, 0.f, 0.f, 0.f};
#pragma unroll
      for (int c = 0; c < 4; ++c) {
        const u32 r0 = 32 * c + 8 * g16 + q;
        const v4s lo = lds_tr(L_V + off_b(r0, 2 * cw + (p >> 1)) + 8 * (p & 1));
        const v4s hi = lds_tr(L_V + off_b(r0 + 4, 2 * cw + (p >> 1)) + 8 * (p & 1));
        acc = mfma32(cat8(lo, hi), PB[c], acc);
      }
      if (c16 < 2) gst(rws, WS_OP + (g * 16 + s) * OPS + c16 * 512 + (16 * cw + 4 * g16) * 4, pack4(acc, t10));
    }
    STAMP(7);
    if (role == 3) {
      const int r = (j - 27) * 8 + x;
      if (r >= 9 || cw != 0) { STAMP_FLUSH(); return; }
      const v4f hs = gather_rows<8>(rws, WS_HP + r * 256, lane, t10);
      const v2u xr = lds_ld<v2u>(L_XR + (64 * r + 4 * c16) * 2);
      float h[4];
      h[0] = rbf(bf_lo(xr.x) + rbf(hs[0])); h[1] = rbf(bf_hi(xr.x) + rbf(hs[1]));
      h[2] = rbf(bf_lo(xr.y) + rbf(hs[2])); h[3] = rbf(bf_hi(xr.y) + rbf(hs[3]));
      STAMP(8);
      const v4f fs = gather_rows<8>(rws, WS_FP + r * 256, lane, t10);
      STAMP(9);
      if (lane < 16) {
        const v2u o = {pk_bf(h[0] + rbf(fs[0]), h[1] + rbf(fs[1])), pk_bf(h[2] + rbf(fs[2]), h[3] + rbf(fs[3]))};
        *(v2u*)((char*)a.out + (64 * r + 4 * c16) * 2) = o;
      }
      if (r == 0 && lane == 0) __builtin_amdgcn_raw_buffer_store_b32(tag32, rws, WS_EP, 0, 16);
      STAMP(10);
      STAMP_FLUSH();
      if (r == 0) AMP_SPIN();
      return;
    }
  }

  const u32 t10 = tag32 & 0x3ffu;
  const int g16 = lane >> 4, c16 = lane & 15;
  wait_flag4(F_S);
  STAMP(8);
  v8b A[4][2];
  {
    const u32 sb = L_S + cw * 8192;
#pragma unroll
    for (int nb = 0; nb < 4; ++nb)
#pragma unroll
      for (int c = 0; c < 2; ++c)
        A[nb][c] = cat8(lds_tr(sb + t128_tr(lane, 0, nb) + c * 4096), lds_tr(sb + t128_tr(lane, 1, nb) + c * 4096));
  }
  u32 vec;
  if (role == 0) {
    const int gg = 2 * x + (cw >> 2), hh = (cw >> 1) & 1;
    const u32 ob = WS_OP + (gg * 16 + 4 * g16) * OPS;
    v4f o[4], st[4];
#pragma unroll 1
    for (u32 it = 0;; ++it) {
      cbar();
      u32 bad = 0;
      v4u w[4], u[4];
#pragma unroll
      for (int i = 0; i < 4; ++i) { w[i] = gld(rws, ob + i * OPS + hh * 512 + (cw & 1) * 256 + c16 * 16); u[i] = gld(rws, ob + i * OPS + 1024); }
#pragma unroll
      for (int i = 0; i < 4; ++i) { o[i] = unpack4(w[i], t10, bad); st[i] = unpack4(u[i], t10, bad); }
      if (all_ok(bad) || it >= SPIN_MAX) break;
    }
    STAMP(9);
    float m[4], l[4];
#pragma unroll
    for (int i = 0; i < 4; ++i) { m[i] = hh ? st[i][2] : st[i][0]; l[i] = hh ? st[i][3] : st[i][1]; }
    float M = fmaxf(fmaxf(m[0], m[1]), fmaxf(m[2], m[3]));
    M = x32_max(x16_max(M));
    float L = 0.f;
    v4f acc = {0.f, 0.f, 0.f, 0.f};
#pragma unroll
    for (int i = 0; i < 4; ++i) {
      const float e = __builtin_amdgcn_exp2f((m[i] - M) * 1.4426950408889634f);
      L += e * l[i];
      acc += e * o[i];
    }
    L = x32_sum(x16_sum(L));
    const float rl = __builtin_amdgcn_rcpf(L);
#pragma unroll
    for (int i = 0; i < 4; ++i) acc[i] = x32_sum(x16_sum(acc[i])) * rl;
    vec = L_VEC + cw * 128;
    if (lane < 16) lds_st<v2u>(vec + c16 * 8, (v2u){pk_bf(acc[0], acc[1]), pk_bf(acc[2], acc[3])});
  } else if (role == 1) {
    const v4f hs = gather_rows<8>(rws, WS_HP + cw * 256, lane, t10);
    STAMP(9);
    const v2u xr = lds_ld<v2u>(L_XR + (64 * cw + 4 * c16) * 2);
    const u32 h01 = pk_bf(bf_lo(xr.x) + rbf(hs[0]), bf_hi(xr.x) + rbf(hs[1]));
    const u32 h23 = pk_bf(bf_lo(xr.y) + rbf(hs[2]), bf_hi(xr.y) + rbf(hs[3]));
    const float h0 = bf_lo(h01), h1 = bf_hi(h01), h2 = bf_lo(h23), h3 = bf_hi(h23);
    const float mw = r16_sum((h0 + h1) + (h2 + h3)) * (1.0f / 64.0f);
    const float d0 = h0 - mw, d1 = h1 - mw, d2 = h2 - mw, d3 = h3 - mw;
    const float m2 = r16_sum((d0 * d0 + d1 * d1) + (d2 * d2 + d3 * d3));
    if (lane < 16) lds_st<v2u>(L_H + (64 * cw + 4 * c16) * 2, (v2u){h01, h23});
    if (lane == 0) lds_st<v2f>(L_ST + cw * 8, (v2f){mw, m2});
    wg_barrier();
    const v2f sw = lds_ld<v2f>(L_ST + (c16 < 9 ? c16 : 0) * 8);
    const u32 hv = lds_ld<unsigned short>(L_H + (64 * jj + lane) * 2);
    const float mean = r16_sum(c16 < 9 ? sw[0] : 0.f) * (1.0f / 9.0f);
    const float dm = sw[0] - mean;
    const float var = r16_sum(c16 < 9 ? sw[1] + 64.0f * dm * dm : 0.f) * (1.0f / 576.0f);
    const float den = rbf(__builtin_amdgcn_sqrtf(rbf(rbf(var) + 1e-5f)));
    const float xn = rbf(bf_lo(hv) - rbf(mean)) * __builtin_amdgcn_rcpf(den);
    vec = L_XN2 + cw * 128;
    lds_st<unsigned short>(vec + lane * 2, (unsigned short)pk_bf(xn, xn));
  } else {
    const v4f ps = gather_rows<9>(rws, WS_PP + x * 9 * 2304 + cw * 256, lane, t10);
    STAMP(9);
    float gv[4];
#pragma unroll
    for (int i = 0; i < 4; ++i) {
      const float pre = rbf(ps[i]);
      gv[i] = pre * __builtin_amdgcn_rcpf(1.0f + __builtin_amdgcn_exp2f(pre * -1.4426950408889634f));
    }
    vec = L_VEC + cw * 128;
    if (lane < 16) lds_st<v2u>(vec + c16 * 8, (v2u){pk_bf(gv[0], gv[1]), pk_bf(gv[2], gv[3])});
  }
  v4f r;
  {
    const v8b B0 = __builtin_bit_cast(v8b, lds_ld<v4u>(vec + g16 * 16)), B1 = __builtin_bit_cast(v8b, lds_ld<v4u>(vec + 64 + g16 * 16));
    const v4f z = {0.f, 0.f, 0.f, 0.f};
    const v4f c0 = mfma32(A[0][1], B1, mfma32(A[0][0], B0, z)), c1 = mfma32(A[1][1], B1, mfma32(A[1][0], B0, z));
    const v4f c2 = mfma32(A[2][1], B1, mfma32(A[2][0], B0, z)), c3 = mfma32(A[3][1], B1, mfma32(A[3][0], B0, z));
    r = pick4(c0, c1, c2, c3, c16);
  }
  if (role == 1) {
    if (c16 < 4) gst(rws, WS_PP + (x * 9 + jj) * 2304 + cw * 256 + (16 * c16 + 4 * g16) * 4, pack4(r, t10));
    STAMP(10);
    STAMP_FLUSH();
    return;
  }
  if (c16 < 4) lds_st<v4f>(L_PART + cw * 256 + (16 * c16 + 4 * g16) * 4, r);
  wg_barrier();
  if (cw == 0) {
    v4f t = lds_ld<v4f>(L_PART + g16 * 256 + c16 * 16) + lds_ld<v4f>(L_PART + (g16 + 4) * 256 + c16 * 16);
    if (role == 2) { const v4f e = lds_ld<v4f>(L_PART + 8 * 256 + c16 * 16); if (g16 == 0) t += e; }
#pragma unroll
    for (int i = 0; i < 4; ++i) t[i] = x32_sum(x16_sum(t[i]));
    if (lane < 16) gst(rws, (role == 0 ? WS_HP : WS_FP) + x * 2304 + jj * 256 + c16 * 16, pack4(t, t10));
  }
  STAMP(10);
  STAMP_FLUSH();
}

}

extern "C" void kernel_launch(void* const* d_in, const int* in_sizes, int n_in, void* d_out, int out_size, void* d_ws, size_t ws_size,
                              hipStream_t stream) {
  (void)in_sizes; (void)n_in; (void)out_size; (void)ws_size;
  Args a;
  a.x = d_in[0]; a.kc = d_in[1]; a.vc = d_in[2]; a.wq = d_in[3]; a.wo = d_in[4]; a.w1 = d_in[6]; a.w2 = d_in[7];
  a.out = d_out; a.ws = d_ws;
  (void)hipFuncSetAttribute((const void*)fused_decode_kernel, hipFuncAttributeMaxDynamicSharedMemorySize, (int)L_END);
  hipLaunchKernelGGL(fused_decode_kernel, dim3(256), dim3(768), L_END, stream, a);
}
```

```cpp
#include <hip/hip_runtime.h>
#include <stdint.h>
#include <type_traits>

namespace {

#define LAS __attribute__((address_space(3)))
typedef unsigned int u32;
typedef u32 v4u __attribute__((ext_vector_type(4)));
typedef u32 v2u __attribute__((ext_vector_type(2)));
typedef float v4f __attribute__((ext_vector_type(4)));
typedef float v2f __attribute__((ext_vector_type(2)));
typedef short v4s __attribute__((ext_vector_type(4)));
typedef short v8s __attribute__((ext_vector_type(8)));
typedef __bf16 v8b __attribute__((ext_vector_type(8)));
typedef __bf16 v2b __attribute__((ext_vector_type(2)));
typedef __amdgpu_buffer_rsrc_t rsrc_t;

#define WIN 10
#ifndef NV1
#define NV1 4
#endif
#define BULK_AUX 1

constexpr u32 L_WQ = 0, L_K = 18432, L_V = 51200, L_S = 83968, L_FL = 157696;
constexpr u32 L_XN = L_FL + 128, L_SC = L_XN + 1152, L_Y = L_SC + 1024, L_END = 163840;
constexpr u32 L_QST = 0, L_QV = 8192, L_P = L_QV + 8 * 576;
constexpr u32 F_WQ = L_FL, F_Q = L_FL + 16, F_V = L_FL + 32, F_S = L_FL + 48, F_QD = L_FL + 64, F_SC = L_FL + 80, F_TAG = L_FL + 96;
constexpr u32 L_XR = L_Y, L_ST = L_Y + 1152, L_H = L_ST + 128, L_XN2 = L_H + 1152;
constexpr u32 L_VEC = L_Y, L_PART = L_Y + 1152;
static_assert(L_XN2 + 9 * 128 <= L_END && L_PART + 9 * 256 <= L_END, "lds");
constexpr u32 WS_EP = 0, WS_QP = 4096, WS_OP = WS_QP + 16 * 8192, OPS = 1152, WS_HP = WS_OP + 256 * OPS;
constexpr u32 WS_PP = WS_HP + 8 * 2304, WS_FP = WS_PP + 72 * 2304, WS_END = WS_FP + 8 * 2304;
constexpr u32 SPIN_MAX = 1u << 16;

struct Args {
  const void* x; const void* kc; const void* vc; const void* wq; const void* wo; const void* w1; const void* w2;
  void* out; void* ws;
};

template <int N, class F> __device__ __forceinline__ void static_for(F&& f) {
  if constexpr (N > 0) { static_for<N - 1>(f); f(std::integral_constant<int, N - 1>{}); }
}

__device__ __forceinline__ float bf_lo(u32 u) { return __uint_as_float(u << 16); }
__device__ __forceinline__ float bf_hi(u32 u) { return __uint_as_float(u & 0xffff0000u); }
__device__ __forceinline__ u32 pk_bf(float lo, float hi) {
  v2b r = __builtin_convertvector((v2f){lo, hi}, v2b);
  return __builtin_bit_cast(u32, r);
}
__device__ __forceinline__ float rbf(float f) { return bf_lo(pk_bf(f, f)); }

template <int C> __device__ __forceinline__ float dppf(float v) {
  return __builtin_bit_cast(float, __builtin_amdgcn_update_dpp(0, __builtin_bit_cast(int, v), C, 0xf, 0xf, true));
}
__device__ __forceinline__ float r16_sum(float v) { v += dppf<0xB1>(v); v += dppf<0x4E>(v); v += dppf<0x141>(v); v += dppf<0x140>(v); return v; }
__device__ __forceinline__ float r16_max(float v) { v = fmaxf(v, dppf<0xB1>(v)); v = fmaxf(v, dppf<0x4E>(v)); v = fmaxf(v, dppf<0x141>(v)); v = fmaxf(v, dppf<0x140>(v)); return v; }
__device__ __forceinline__ float x16_sum(float v) { auto r = __builtin_amdgcn_permlane16_swap(__float_as_uint(v), __float_as_uint(v), false, false); return __uint_as_float(r[0]) + __uint_as_float(r[1]); }
__device__ __forceinline__ float x32_sum(float v) { auto r = __builtin_amdgcn_permlane32_swap(__float_as_uint(v), __float_as_uint(v), false, false); return __uint_as_float(r[0]) + __uint_as_float(r[1]); }
__device__ __forceinline__ float x16_max(float v) { auto r = __builtin_amdgcn_permlane16_swap(__float_as_uint(v), __float_as_uint(v), false, false); return fmaxf(__uint_as_float(r[0]), __uint_as_float(r[1])); }
__device__ __forceinline__ float x32_max(float v) { auto r = __builtin_amdgcn_permlane32_swap(__float_as_uint(v), __float_as_uint(v), false, false); return fmaxf(__uint_as_float(r[0]), __uint_as_float(r[1])); }

template <class T> __device__ __forceinline__ T lds_ld(u32 a) { return *(const LAS T*)(size_t)a; }
template <class T> __device__ __forceinline__ void lds_st(u32 a, T v) { *(LAS T*)(size_t)a = v; }
__device__ __forceinline__ v4s lds_tr(u32 a) { return __builtin_amdgcn_ds_read_tr16_b64_v4i16((LAS v4s*)(size_t)a); }
__device__ __forceinline__ v8b cat8(v4s a, v4s b) { return __builtin_bit_cast(v8b, __builtin_shufflevector(a, b, 0, 1, 2, 3, 4, 5, 6, 7)); }
__device__ __forceinline__ v4f mfma32(v8b a, v8b b, v4f c) { return __builtin_amdgcn_mfma_f32_16x16x32_bf16(a, b, c, 0, 0, 0); }

__device__ __forceinline__ void lgkm0() { asm volatile("s_waitcnt lgkmcnt(0)" ::: "memory"); }
template <int N> __device__ __forceinline__ void vmwait() { asm volatile("s_waitcnt vmcnt(%0)" ::"n"(N) : "memory"); }
__device__ __forceinline__ void cbar() { asm volatile("" ::: "memory"); }
__device__ __forceinline__ void wg_barrier() { lgkm0(); __builtin_amdgcn_s_barrier(); cbar(); }

template <int AUX> __device__ __forceinline__ void dma(rsrc_t r, u32 voff, u32 soff, u32 lds) {
  if constexpr (AUX == 1) asm volatile("s_mov_b32 m0, %0\n\ts_nop 0\n\tbuffer_load_dwordx4 %1, %2, %3 offen nt lds" ::"s"(lds), "v"(voff), "s"(r), "s"(soff) : "memory");
  else if constexpr (AUX == 16) asm volatile("s_mov_b32 m0, %0\n\ts_nop 0\n\tbuffer_load_dwordx4 %1, %2, %3 offen sc1 lds" ::"s"(lds), "v"(voff), "s"(r), "s"(soff) : "memory");
  else asm volatile("s_mov_b32 m0, %0\n\ts_nop 0\n\tbuffer_load_dwordx4 %1, %2, %3 offen lds" ::"s"(lds), "v"(voff), "s"(r), "s"(soff) : "memory");
}

__device__ __forceinline__ void set_flag(u32 a, int lane) {
  if (lane == 0) *(volatile LAS u32*)(size_t)a = 1u;
}
__device__ __forceinline__ void wait_flag4(u32 a) {
#pragma unroll 1
  for (u32 it = 0; it < (1u << 22); ++it) {
    v4u v;
    asm volatile("ds_read_b128 %0, %1\n\ts_waitcnt lgkmcnt(0)" : "=v"(v) : "v"(a) : "memory");
    if (__builtin_amdgcn_readfirstlane(v.x & v.y & v.z & v.w)) break;
    __builtin_amdgcn_s_sleep(1);
  }
}
__device__ __forceinline__ void wait_cnt(u32 a, u32 n) {
#pragma unroll 1
  for (u32 it = 0; it < (1u << 22); ++it) {
    u32 v;
    asm volatile("ds_read_b32 %0, %1\n\ts_waitcnt lgkmcnt(0)" : "=v"(v) : "v"(a) : "memory");
    if (__builtin_amdgcn_readfirstlane(v) >= n) break;
    __builtin_amdgcn_s_sleep(1);
  }
}

__device__ __forceinline__ v4u gld(rsrc_t r, u32 off) { return __builtin_amdgcn_raw_buffer_load_b128(r, off, 0, 16); }
__device__ __forceinline__ void gst(rsrc_t r, u32 off, v4u v) { __builtin_amdgcn_raw_buffer_store_b128(v, r, off, 0, 16); }
__device__ __forceinline__ v4u pack4(v4f v, u32 t) {
  v4u r;
#pragma unroll
  for (int i = 0; i < 4; ++i) r[i] = ((__float_as_uint(v[i]) + 0x200u) & ~0x3ffu) | t;
  return r;
}
__device__ __forceinline__ v4f unpack4(v4u w, u32 t, u32& bad) {
  v4u y = w ^ t;
  bad |= y.x | y.y | y.z | y.w;
  return __builtin_bit_cast(v4f, y);
}
__device__ __forceinline__ bool all_ok(u32 bad) { return __all((bad & 0x3ffu) == 0u); }

#define STAMP(i) do { } while (0)
#define STAMP_DECL do { } while (0)
#define STAMP_FLUSH() do { } while (0)
#define AMP_SPIN() do { } while (0)

__device__ __forceinline__ u32 t128_tr(int lane, int t, int nb) {
  const int g = lane >> 4, q = (lane & 15) >> 2, p = lane & 3;
  const int r = 8 * g + 4 * t + q;
  const int f = (q >> 1) | (((t ^ g) & 1) << 1);
  return (u32)(r * 128 + ((nb ^ f) << 5) + ((p >> 1) << 4) + ((p & 1) << 3));
}
__device__ __forceinline__ u32 off_b(u32 row, u32 ch) { return 256u * row + 16u * (ch ^ (((row & 3) << 2) | ((row >> 2) & 3))); }

template <int NP> __device__ __forceinline__ v4f gather_rows(rsrc_t rws, u32 base, int lane, u32 t) {
  const int pq = lane >> 4, q = lane & 15;
  const u32 o = base + pq * 2304 + q * 16;
  v4f s;
#pragma unroll 1
  for (u32 it = 0;; ++it) {
    cbar();
    u32 bad = 0;
    v4u w0 = gld(rws, o), w1 = gld(rws, o + 4 * 2304), w2;
    if constexpr (NP == 9) w2 = gld(rws, base + 8 * 2304 + q * 16);
    s = unpack4(w0, t, bad) + unpack4(w1, t, bad);
    if constexpr (NP == 9) { v4f e = unpack4(w2, t, bad); if (pq == 0) s += e; }
    if (all_ok(bad) || it >= SPIN_MAX) break;
  }
#pragma unroll
  for (int i = 0; i < 4; ++i) s[i] = x32_sum(x16_sum(s[i]));
  return s;
}

__device__ __forceinline__ v4f pick4(v4f a0, v4f a1, v4f a2, v4f a3, int c16) {
  const bool b0 = c16 & 1, b1 = c16 & 2;
  v4f r;
#pragma unroll
  for (int i = 0; i < 4; ++i) {
    const float lo = b0 ? a1[i] : a0[i], hi = b0 ? a3[i] : a2[i];
    r[i] = b1 ? hi : lo;
  }
  return r;
}

__global__ void __launch_bounds__(768, 3) fused_decode_kernel(Args a) {
  extern __shared__ __attribute__((aligned(16))) unsigned char smem[];
  const int tid = threadIdx.x, lane = tid & 63;
  const int wv = __builtin_amdgcn_readfirstlane(tid >> 6);
  const int b = blockIdx.x, x = b & 7, j = b >> 3;
  const int g = 2 * x + (j >> 4), s = j & 15;
  const int role = j < 9 ? 0 : (j < 18 ? 1 : (j < 27 ? 2 : 3));
  const int jj = j - 9 * role;
  const rsrc_t rws = __builtin_amdgcn_make_buffer_rsrc(a.ws, 0, WS_END, 0x00020000);
  STAMP_DECL;
  STAMP(0);
  int cw;
  u32 tag32;

  if (wv < 4) {
    const int lw = wv;
    constexpr int W = WIN;
    constexpr int N_WQ = 5, N_K = 13, N_V1 = 13 + NV1, N_QG = N_V1 + 2, N_V2 = 23;
    auto after = [&](auto n_) {
      constexpr int n = decltype(n_)::value;
      if constexpr (n > W) vmwait<W>();
      if constexpr (n - W == N_WQ) set_flag(F_WQ + 4 * lw, lane);
      if constexpr (n - W == N_QG) set_flag(F_Q + 4 * lw, lane);
      if constexpr (n - W == N_V2) set_flag(F_V + 4 * lw, lane);
    };
    const u32 vt = (u32)(((lane & 7) ^ (((lane >> 4) & 1) << 1) ^ (((lane >> 5) & 1) << 2)) * 16);
    {
      const rsrc_t rq = __builtin_amdgcn_make_buffer_rsrc((void*)a.wq, 0, 576 * 4096 * 2, 0x00020000);
      const u32 voff = (u32)(lane >> 3) * 8192u + vt;
      const u32 base = (u32)(s >> 2) * 144u * 8192u + (u32)(g * 256 + (s & 3) * 64) * 2u;
      static_for<5>([&](auto k_) {
        constexpr int k = decltype(k_)::value;
        const int i = min(lw + 4 * k, 17);
        dma<BULK_AUX>(rq, voff ^ (u32)((i & 1) << 6), base + i * 65536u, L_WQ + i * 1024u);
        after(std::integral_constant<int, k + 1>{});
      });
    }
    __builtin_amdgcn_s_barrier();
    cbar();
    const u32 vkv = ((u32)(lane >> 4) * 256u + (u32)(((lane & 15) ^ ((lane >> 4) << 2)) * 16)) ^ (u32)(lw << 4);
    const u32 bkv = (u32)(g * 8192 + 6144 + s * 128) * 256u + lw * 1024u;
    const rsrc_t rk = __builtin_amdgcn_make_buffer_rsrc((void*)a.kc, 0, 16 * 8192 * 256, 0x00020000);
    const rsrc_t rv = __builtin_amdgcn_make_buffer_rsrc((void*)a.vc, 0, 16 * 8192 * 256, 0x00020000);
    static_for<8>([&](auto k_) {
      constexpr int k = decltype(k_)::value;
      dma<BULK_AUX>(rk, vkv, bkv + k * 4096u, L_K + lw * 1024u + k * 4096u);
      after(std::integral_constant<int, N_WQ + k + 1>{});
    });
    static_for<NV1>([&](auto k_) {
      constexpr int k = decltype(k_)::value;
      dma<BULK_AUX>(rv, vkv, bkv + k * 4096u, L_V + lw * 1024u + k * 4096u);
      after(std::integral_constant<int, N_K + k + 1>{});
    });
    vmwait<NV1>();
    if constexpr (N_V1 - W < N_WQ) set_flag(F_WQ + 4 * lw, lane);
    STAMP(1);
    wait_flag4(F_QD);
    static_for<2>([&](auto k_) {
      constexpr int k = decltype(k_)::value;
      dma<16>(rws, (u32)lane * 16u, WS_QP + g * 8192u + (lw + 4 * k) * 1024u, L_QST + (lw + 4 * k) * 1024u);
      after(std::integral_constant<int, N_V1 + k + 1>{});
    });
    static_for<8 - NV1>([&](auto k_) {
      constexpr int k = decltype(k_)::value;
      dma<BULK_AUX>(rv, vkv, bkv + (NV1 + k) * 4096u, L_V + lw * 1024u + (NV1 + k) * 4096u);
      after(std::integral_constant<int, N_QG + k + 1>{});
    });
    if (role < 3) {
      const void* wp = role == 0 ? a.wo : (role == 1 ? a.w1 : a.w2);
      const u32 wbytes = role == 0 ? 4096u * 576u * 2u : 4608u * 576u * 2u;
      const rsrc_t rw = __builtin_amdgcn_make_buffer_rsrc((void*)wp, 0, wbytes, 0x00020000);
      const u32 rs = role == 1 ? 9216u : 1152u;
      const u32 base = role == 0 ? (u32)x * 512u * 1152u + (u32)jj * 128u
                     : role == 1 ? (u32)jj * 64u * 9216u + (u32)x * 1152u
                                 : (u32)x * 576u * 1152u + (u32)jj * 128u;
      const int nk = role == 0 ? 16 : 18;
      const u32 voff = (u32)(lane >> 3) * rs + vt;
      static_for<18>([&](auto k_) {
        constexpr int k = decltype(k_)::value;
        if (k < nk) {
          const u32 i = lw + 4 * k;
          const u32 rb = role == 1 ? (i * 7282u) >> 16 : i;
          const u32 p = role == 1 ? i - rb * 9u : 0u;
          dma<BULK_AUX>(rw, voff ^ ((rb & 1u) << 6), base + rb * 8u * rs + p * 128u, L_S + (p * 8u + rb) * 1024u);
          after(std::integral_constant<int, N_V2 + k + 1>{});
        }
      });
      static_assert(N_V2 + W <= N_V2 + 16, "flags must be inside the guarded range");
    } else {
      if constexpr (N_V2 - W < N_QG) { vmwait<8 - NV1>(); set_flag(F_Q + 4 * lw, lane); }
      vmwait<0>();
      set_flag(F_V + 4 * lw, lane);
    }
    vmwait<0>();
    set_flag(F_S + 4 * lw, lane);
    STAMP(2);
    if (lw != 0 || role == 0 || role == 3) { STAMP_FLUSH(); return; }
    lgkm0();
    tag32 = lds_ld<u32>(F_TAG);
    cw = 8;
  } else {
    cw = wv - 4;
    const u32 E = __builtin_amdgcn_raw_buffer_load_b32(rws, WS_EP, 0, 16);
    v4u xa, xb;
    if (cw < 4) {
      const rsrc_t rx = __builtin_amdgcn_make_buffer_rsrc((void*)a.x, 0, 1152, 0x00020000);
      xa = __builtin_amdgcn_raw_buffer_load_b128(rx, lane * 16, 0, 0);
      xb = __builtin_amdgcn_raw_buffer_load_b128(rx, 1024 + lane * 16, 0, 0);
    }
    if (cw == 7 && lane < 32) lds_st<u32>(L_FL + lane * 4, 0u);
    wg_barrier();
    tag32 = __builtin_amdgcn_readfirstlane(E) + 1u;
    const int g16 = lane >> 4, c16 = lane & 15;
    if (cw < 4) {
      float v[16];
#pragma unroll
      for (int i = 0; i < 4; ++i) { v[2 * i] = bf_lo(xa[i]); v[2 * i + 1] = bf_hi(xa[i]); v[8 + 2 * i] = bf_lo(xb[i]); v[9 + 2 * i] = bf_hi(xb[i]); }
      float sm = 0.f;
#pragma unroll
      for (int i = 0; i < 16; ++i) sm += v[i];
      sm = x32_sum(x16_sum(r16_sum(sm)));
      const float mean = sm * (1.0f / 576.0f);
      float s1 = 0.f, s2 = 0.f;
#pragma unroll
      for (int i = 0; i < 8; ++i) { const float d = v[i] - mean, e = v[8 + i] - mean; s1 += d * d; s2 += e * e; }
      if (lane < 8) s1 += s2;
      s1 = x32_sum(x16_sum(r16_sum(s1)));
      const float m = rbf(mean);
      const float den = rbf(__builtin_amdgcn_sqrtf(rbf(rbf(s1 * (1.0f / 576.0f)) + 1e-5f)));
      const float rden = __builtin_amdgcn_rcpf(den);
      v4u na, nb;
#pragma unroll
      for (int i = 0; i < 4; ++i) {
        const u32 d0 = pk_bf(v[2 * i] - m, v[2 * i + 1] - m), d1 = pk_bf(v[8 + 2 * i] - m, v[9 + 2 * i] - m);
        na[i] = pk_bf(bf_lo(d0) * rden, bf_hi(d0) * rden);
        nb[i] = pk_bf(bf_lo(d1) * rden, bf_hi(d1) * rden);
      }
      lds_st<v4u>(L_XN + lane * 16, na);
      lds_st<v4u>(L_XR + lane * 16, xa);
      if (lane < 8) { lds_st<v4u>(L_XN + 1024 + lane * 16, nb); lds_st<v4u>(L_XR + 1024 + lane * 16, xb); }
      if (cw == 0 && lane == 0) lds_st<u32>(F_TAG, tag32);
      lgkm0();
      STAMP(1);
      wait_flag4(F_WQ);
      STAMP(2);
      const int ks = s >> 2;
      const u32 a0 = L_WQ + t128_tr(lane, 0, cw), a1 = L_WQ + t128_tr(lane, 1, cw);
      v4f acc = {0.f, 0.f, 0.f, 0.f};
#pragma unroll
      for (int c = 0; c < 4; ++c) {
        const v8b A = cat8(lds_tr(a0 + c * 4096), lds_tr(a1 + c * 4096));
        const v8b B = __builtin_bit_cast(v8b, lds_ld<v4u>(L_XN + (ks * 18 + 4 * c + g16) * 16));
        acc = mfma32(A, B, acc);
      }
      {
        const int q = (lane & 15) >> 2, p = lane & 3, r = 128 + 4 * g16 + q;
        const int f = (q >> 1) | (((g16 ^ (g16 >> 1)) & 1) << 1);
        const v4s A = lds_tr(L_WQ + r * 128 + ((cw ^ f) << 5) + ((p >> 1) << 4) + ((p & 1) << 3));
        const v4s B = __builtin_bit_cast(v4s, lds_ld<v2u>(L_XN + (ks * 144 + 128 + 4 * g16) * 2));
        acc = __builtin_amdgcn_mfma_f32_16x16x16bf16_1k(A, B, acc, 0, 0, 0);
      }
      if (c16 < 2) {
        const float v0 = c16 ? acc[2] : acc[0], v1 = c16 ? acc[3] : acc[1];
        gst(rws, WS_QP + g * 8192u + (u32)(ks * 128 + (s & 3) * 32 + cw * 8 + 2 * g16 + c16) * 16u,
            (v4u){__float_as_uint(v0), tag32, __float_as_uint(v1), tag32});
      }
      lgkm0();
      set_flag(F_QD + 4 * cw, lane);
      STAMP(3);
    }
    const u32 t10 = tag32 & 0x3ffu;
    wait_flag4(F_Q);
    STAMP(4);
    {
      float q0[2] = {0.f, 0.f}, q1[2] = {0.f, 0.f};
      u32 bad = 0;
#pragma unroll
      for (int k = 0; k < 4; ++k) {
        const v4u u0 = lds_ld<v4u>(L_QST + (k * 128 + lane) * 16), u1 = lds_ld<v4u>(L_QST + (k * 128 + 64 + lane) * 16);
        q0[0] += __uint_as_float(u0.x); q0[1] += __uint_as_float(u0.z);
        q1[0] += __uint_as_float(u1.x); q1[1] += __uint_as_float(u1.z);
        bad |= (u0.y ^ tag32) | (u0.w ^ tag32) | (u1.y ^ tag32) | (u1.w ^ tag32);
      }
      if (!__all(bad == 0u)) {
#pragma unroll 1
        for (u32 it = 0; it < SPIN_MAX; ++it) {
          cbar();
          bad = 0; q0[0] = q0[1] = q1[0] = q1[1] = 0.f;
#pragma unroll
          for (int k = 0; k < 4; ++k) {
            const v4u u0 = gld(rws, WS_QP + g * 8192u + (k * 128 + lane) * 16), u1 = gld(rws, WS_QP + g * 8192u + (k * 128 + 64 + lane) * 16);
            q0[0] += __uint_as_float(u0.x); q0[1] += __uint_as_float(u0.z);
            q1[0] += __uint_as_float(u1.x); q1[1] += __uint_as_float(u1.z);
            bad |= (u0.y ^ tag32) | (u0.w ^ tag32) | (u1.y ^ tag32) | (u1.w ^ tag32);
          }
          if (__all(bad == 0u)) break;
        }
        STAMP(15);
      }
      lds_st<u32>(L_QV + cw * 576 + lane * 4, pk_bf(q0[0], q0[1]));
      lds_st<u32>(L_QV + cw * 576 + 272 + lane * 4, pk_bf(q1[0], q1[1]));
    }
    {
      const u32 key = 16 * cw + c16;
      v4f acc = {0.f, 0.f, 0.f, 0.f};
#pragma unroll
      for (int c = 0; c < 4; ++c) {
        const v8b A = __builtin_bit_cast(v8b, lds_ld<v4u>(L_K + off_b(key, 4 * c + g16)));
        const v8b B = __builtin_bit_cast(v8b, lds_ld<v4u>(L_QV + cw * 576 + (lane & 1) * 272 + (32 * c + 8 * g16) * 2));
        acc = mfma32(A, B, acc);
      }
      v4f sc;
#pragma unroll
      for (int i = 0; i < 4; ++i) sc[i] = rbf(rbf(acc[i]) * 0.08838834764831845f);
      if (c16 < 2) lds_st<v4f>(L_SC + c16 * 512 + (16 * cw + 4 * g16) * 4, sc);
      lgkm0();
      if (lane == 0) __hip_atomic_fetch_add((LAS u32*)(size_t)F_SC, 1u, __ATOMIC_RELAXED, __HIP_MEMORY_SCOPE_WORKGROUP);
      wait_cnt(F_SC, 8);
    }
    STAMP(5);
    {
      const v4f sc = lds_ld<v4f>(L_SC + (lane >> 5) * 512 + (lane & 31) * 16);
      float m = fmaxf(fmaxf(sc[0], sc[1]), fmaxf(sc[2], sc[3]));
      m = x16_max(r16_max(m));
      v4f e;
#pragma unroll
      for (int i = 0; i < 4; ++i) e[i] = __builtin_amdgcn_exp2f((sc[i] - m) * 1.4426950408889634f);
      float l = (e[0] + e[1]) + (e[2] + e[3]);
      l = x16_sum(r16_sum(l));
      lds_st<v2u>(L_P + cw * 576 + (lane >> 5) * 272 + (lane & 31) * 8, (v2u){pk_bf(e[0], e[1]), pk_bf(e[2], e[3])});
      if (cw == 0) {
        const float m1 = __uint_as_float(__builtin_amdgcn_readlane(__float_as_uint(m), 32)), l1 = __uint_as_float(__builtin_amdgcn_readlane(__float_as_uint(l), 32));
        if (lane == 0) gst(rws, WS_OP + (g * 16 + s) * OPS + 1024, pack4((v4f){m, l, m1, l1}, t10));
      }
    }
    v8b PB[4];
#pragma unroll
    for (int c = 0; c < 4; ++c) PB[c] = __builtin_bit_cast(v8b, lds_ld<v4u>(L_P + cw * 576 + (lane & 1) * 272 + (32 * c + 8 * g16) * 2));
    wait_flag4(F_V);
    STAMP(6);
    {
      const int q = (lane & 15) >> 2, p = lane & 3;
      v4f acc = {0.f, 0.f, 0.f, 0.f};
#pragma unroll
      for (int c = 0; c < 4; ++c) {
        const u32 r0 = 32 * c + 8 * g16 + q;
        const v4s lo = lds_tr(L_V + off_b(r0, 2 * cw + (p >> 1)) + 8 * (p & 1));
        const v4s hi = lds_tr(L_V + off_b(r0 + 4, 2 * cw + (p >> 1)) + 8 * (p & 1));
        acc = mfma32(cat8(lo, hi), PB[c], acc);
      }
      if (c16 < 2) gst(rws, WS_OP + (g * 16 + s) * OPS + c16 * 512 + (16 * cw + 4 * g16) * 4, pack4(acc, t10));
    }
    STAMP(7);
    if (role == 3) {
      const int r = (j - 27) * 8 + x;
      if (r >= 9 || cw != 0) { STAMP_FLUSH(); return; }
      const v4f hs = gather_rows<8>(rws, WS_HP + r * 256, lane, t10);
      const v2u xr = lds_ld<v2u>(L_XR + (64 * r + 4 * c16) * 2);
      float h[4];
      h[0] = rbf(bf_lo(xr.x) + rbf(hs[0])); h[1] = rbf(bf_hi(xr.x) + rbf(hs[1]));
      h[2] = rbf(bf_lo(xr.y) + rbf(hs[2])); h[3] = rbf(bf_hi(xr.y) + rbf(hs[3]));
      STAMP(8);
      const v4f fs = gather_rows<8>(rws, WS_FP + r * 256, lane, t10);
      STAMP(9);
      if (lane < 16) {
        const v2u o = {pk_bf(h[0] + rbf(fs[0]), h[1] + rbf(fs[1])), pk_bf(h[2] + rbf(fs[2]), h[3] + rbf(fs[3]))};
        *(v2u*)((char*)a.out + (64 * r + 4 * c16) * 2) = o;
      }
      if (r == 0 && lane == 0) __builtin_amdgcn_raw_buffer_store_b32(tag32, rws, WS_EP, 0, 16);
      STAMP(10);
      STAMP_FLUSH();
      if (r == 0) AMP_SPIN();
      return;
    }
  }

  const u32 t10 = tag32 & 0x3ffu;
  const int g16 = lane >> 4, c16 = lane & 15;
  wait_flag4(F_S);
  STAMP(8);
  v8b A[4][2];
  {
    const u32 sb = L_S + cw * 8192;
#pragma unroll
    for (int nb = 0; nb < 4; ++nb)
#pragma unroll
      for (int c = 0; c < 2; ++c)
        A[nb][c] = cat8(lds_tr(sb + t128_tr(lane, 0, nb) + c * 4096), lds_tr(sb + t128_tr(lane, 1, nb) + c * 4096));
  }
  u32 vec;
  if (role == 0) {
    const int gg = 2 * x + (cw >> 2), hh = (cw >> 1) & 1;
    const u32 ob = WS_OP + (gg * 16 + 4 * g16) * OPS;
    v4f o[4], st[4];
#pragma unroll 1
    for (u32 it = 0;; ++it) {
      cbar();
      u32 bad = 0;
      v4u w[4], u[4];
#pragma unroll
      for (int i = 0; i < 4; ++i) { w[i] = gld(rws, ob + i * OPS + hh * 512 + (cw & 1) * 256 + c16 * 16); u[i] = gld(rws, ob + i * OPS + 1024); }
#pragma unroll
      for (int i = 0; i < 4; ++i) { o[i] = unpack4(w[i], t10, bad); st[i] = unpack4(u[i], t10, bad); }
      if (all_ok(bad) || it >= SPIN_MAX) break;
    }
    STAMP(9);
    float m[4], l[4];
#pragma unroll
    for (int i = 0; i < 4; ++i) { m[i] = hh ? st[i][2] : st[i][0]; l[i] = hh ? st[i][3] : st[i][1]; }
    float M = fmaxf(fmaxf(m[0], m[1]), fmaxf(m[2], m[3]));
    M = x32_max(x16_max(M));
    float L = 0.f;
    v4f acc = {0.f, 0.f, 0.f, 0.f};
#pragma unroll
    for (int i = 0; i < 4; ++i) {
      const float e = __builtin_amdgcn_exp2f((m[i] - M) * 1.4426950408889634f);
      L += e * l[i];
      acc += e * o[i];
    }
    L = x32_sum(x16_sum(L));
    const float rl = __builtin_amdgcn_rcpf(L);
#pragma unroll
    for (int i = 0; i < 4; ++i) acc[i] = x32_sum(x16_sum(acc[i])) * rl;
    vec = L_VEC + cw * 128;
    if (lane < 16) lds_st<v2u>(vec + c16 * 8, (v2u){pk_bf(acc[0], acc[1]), pk_bf(acc[2], acc[3])});
  } else if (role == 1) {
    const v4f hs = gather_rows<8>(rws, WS_HP + cw * 256, lane, t10);
    STAMP(9);
    const v2u xr = lds_ld<v2u>(L_XR + (64 * cw + 4 * c16) * 2);
    const u32 h01 = pk_bf(bf_lo(xr.x) + rbf(hs[0]), bf_hi(xr.x) + rbf(hs[1]));
    const u32 h23 = pk_bf(bf_lo(xr.y) + rbf(hs[2]), bf_hi(xr.y) + rbf(hs[3]));
    const float h0 = bf_lo(h01), h1 = bf_hi(h01), h2 = bf_lo(h23), h3 = bf_hi(h23);
    const float mw = r16_sum((h0 + h1) + (h2 + h3)) * (1.0f / 64.0f);
    const float d0 = h0 - mw, d1 = h1 - mw, d2 = h2 - mw, d3 = h3 - mw;
    const float m2 = r16_sum((d0 * d0 + d1 * d1) + (d2 * d2 + d3 * d3));
    if (lane < 16) lds_st<v2u>(L_H + (64 * cw + 4 * c16) * 2, (v2u){h01, h23});
    if (lane == 0) lds_st<v2f>(L_ST + cw * 8, (v2f){mw, m2});
    wg_barrier();
    const v2f sw = lds_ld<v2f>(L_ST + (c16 < 9 ? c16 : 0) * 8);
    const u32 hv = lds_ld<unsigned short>(L_H + (64 * jj + lane) * 2);
    const float mean = r16_sum(c16 < 9 ? sw[0] : 0.f) * (1.0f / 9.0f);
    const float dm = sw[0] - mean;
    const float var = r16_sum(c16 < 9 ? sw[1] + 64.0f * dm * dm : 0.f) * (1.0f / 576.0f);
    const float den = rbf(__builtin_amdgcn_sqrtf(rbf(rbf(var) + 1e-5f)));
    const float xn = rbf(bf_lo(hv) - rbf(mean)) * __builtin_amdgcn_rcpf(den);
    vec = L_XN2 + cw * 128;
    lds_st<unsigned short>(vec + lane * 2, (unsigned short)pk_bf(xn, xn));
  } else {
    const v4f ps = gather_rows<9>(rws, WS_PP + x * 9 * 2304 + cw * 256, lane, t10);
    STAMP(9);
    float gv[4];
#pragma unroll
    for (int i = 0; i < 4; ++i) {
      const float pre = rbf(ps[i]);
      gv[i] = pre * __builtin_amdgcn_rcpf(1.0f + __builtin_amdgcn_exp2f(pre * -1.4426950408889634f));
    }
    vec = L_VEC + cw * 128;
    if (lane < 16) lds_st<v2u>(vec + c16 * 8, (v2u){pk_bf(gv[0], gv[1]), pk_bf(gv[2], gv[3])});
  }
  v4f r;
  {
    const v8b B0 = __builtin_bit_cast(v8b, lds_ld<v4u>(vec + g16 * 16)), B1 = __builtin_bit_cast(v8b, lds_ld<v4u>(vec + 64 + g16 * 16));
    const v4f z = {0.f, 0.f, 0.f, 0.f};
    const v4f c0 = mfma32(A[0][1], B1, mfma32(A[0][0], B0, z)), c1 = mfma32(A[1][1], B1, mfma32(A[1][0], B0, z));
    const v4f c2 = mfma32(A[2][1], B1, mfma32(A[2][0], B0, z)), c3 = mfma32(A[3][1], B1, mfma32(A[3][0], B0, z));
    r = pick4(c0, c1, c2, c3, c16);
  }
  if (role == 1) {
    if (c16 < 4) gst(rws, WS_PP + (x * 9 + jj) * 2304 + cw * 256 + (16 * c16 + 4 * g16) * 4, pack4(r, t10));
    STAMP(10);
    STAMP_FLUSH();
    return;
  }
  if (c16 < 4) lds_st<v4f>(L_PART + cw * 256 + (16 * c16 + 4 * g16) * 4, r);
  wg_barrier();
  if (cw == 0) {
    v4f t = lds_ld<v4f>(L_PART + g16 * 256 + c16 * 16) + lds_ld<v4f>(L_PART + (g16 + 4) * 256 + c16 * 16);
    if (role == 2) { const v4f e = lds_ld<v4f>(L_PART + 8 * 256 + c16 * 16); if (g16 == 0) t += e; }
#pragma unroll
    for (int i = 0; i < 4; ++i) t[i] = x32_sum(x16_sum(t[i]));
    if (lane < 16) gst(rws, (role == 0 ? WS_HP : WS_FP) + x * 2304 + jj * 256 + c16 * 16, pack4(t, t10));
  }
  STAMP(10);
  STAMP_FLUSH();
}

__global__ void null_kernel(u32* p) { if (threadIdx.x == 0) p[0] = 1; }
}

extern "C" void kernel_launch(void* const* d_in, const int* in_sizes, int n_in, void* d_out, int out_size, void* d_ws, size_t ws_size,
                              hipStream_t stream) {
  (void)in_sizes; (void)n_in; (void)out_size; (void)ws_size;
  Args a;
  a.x = d_in[0]; a.kc = d_in[1]; a.vc = d_in[2]; a.wq = d_in[3]; a.wo = d_in[4]; a.w1 = d_in[6]; a.w2 = d_in[7];
  a.out = d_out; a.ws = d_ws;
  (void)hipFuncSetAttribute((const void*)fused_decode_kernel, hipFuncAttributeMaxDynamicSharedMemorySize, (int)L_END);
  hipLaunchKernelGGL(fused_decode_kernel, dim3(256), dim3(768), L_END, stream, a);
  hipLaunchKernelGGL(null_kernel, dim3(1), dim3(64), 0, stream, (u32*)d_ws + 65536);
}
```

```cpp
#define AMP 0
#include <hip/hip_runtime.h>
#include <stdint.h>
#include <type_traits>

namespace {

#define LAS __attribute__((address_space(3)))
typedef unsigned int u32;
typedef u32 v4u __attribute__((ext_vector_type(4)));
typedef u32 v2u __attribute__((ext_vector_type(2)));
typedef float v4f __attribute__((ext_vector_type(4)));
typedef float v2f __attribute__((ext_vector_type(2)));
typedef short v4s __attribute__((ext_vector_type(4)));
typedef short v8s __attribute__((ext_vector_type(8)));
typedef __bf16 v8b __attribute__((ext_vector_type(8)));
typedef __bf16 v2b __attribute__((ext_vector_type(2)));
typedef __amdgpu_buffer_rsrc_t rsrc_t;

#define WIN 10
#ifndef NV1
#define NV1 4
#endif
#define BULK_AUX 1

constexpr u32 L_WQ = 0, L_K = 18432, L_V = 51200, L_S = 83968, L_FL = 157696;
constexpr u32 L_XN = L_FL + 128, L_SC = L_XN + 1152, L_Y = L_SC + 1024, L_END = 163840;
constexpr u32 L_QST = 0, L_QV = 8192, L_P = L_QV + 8 * 576;
constexpr u32 F_WQ = L_FL, F_Q = L_FL + 16, F_V = L_FL + 32, F_S = L_FL + 48, F_QD = L_FL + 64, F_SC = L_FL + 80, F_TAG = L_FL + 96;
constexpr u32 L_XR = L_Y, L_ST = L_Y + 1152, L_H = L_ST + 128, L_XN2 = L_H + 1152;
constexpr u32 L_VEC = L_Y, L_PART = L_Y + 1152;
static_assert(L_XN2 + 9 * 128 <= L_END && L_PART + 9 * 256 <= L_END, "lds");
constexpr u32 WS_EP = 0, WS_QP = 4096, WS_OP = WS_QP + 16 * 8192, OPS = 1152, WS_HP = WS_OP + 256 * OPS;
constexpr u32 WS_PP = WS_HP + 8 * 2304, WS_FP = WS_PP + 72 * 2304, WS_END = WS_FP + 8 * 2304;
constexpr u32 SPIN_MAX = 1u << 16;

struct Args {
  const void* x; const void* kc; const void* vc; const void* wq; const void* wo; const void* w1; const void* w2;
  void* out; void* ws;
};

template <int N, class F> __device__ __forceinline__ void static_for(F&& f) {
  if constexpr (N > 0) { static_for<N - 1>(f); f(std::integral_constant<int, N - 1>{}); }
}

__device__ __forceinline__ float bf_lo(u32 u) { return __uint_as_float(u << 16); }
__device__ __forceinline__ float bf_hi(u32 u) { return __uint_as_float(u & 0xffff0000u); }
__device__ __forceinline__ u32 pk_bf(float lo, float hi) {
  v2b r = __builtin_convertvector((v2f){lo, hi}, v2b);
  return __builtin_bit_cast(u32, r);
}
__device__ __forceinline__ float rbf(float f) { return bf_lo(pk_bf(f, f)); }

template <int C> __device__ __forceinline__ float dppf(float v) {
  return __builtin_bit_cast(float, __builtin_amdgcn_update_dpp(0, __builtin_bit_cast(int, v), C, 0xf, 0xf, true));
}
__device__ __forceinline__ float r16_sum(float v) { v += dppf<0xB1>(v); v += dppf<0x4E>(v); v += dppf<0x141>(v); v += dppf<0x140>(v); return v; }
__device__ __forceinline__ float r16_max(float v) { v = fmaxf(v, dppf<0xB1>(v)); v = fmaxf(v, dppf<0x4E>(v)); v = fmaxf(v, dppf<0x141>(v)); v = fmaxf(v, dppf<0x140>(v)); return v; }
__device__ __forceinline__ float x16_sum(float v) { auto r = __builtin_amdgcn_permlane16_swap(__float_as_uint(v), __float_as_uint(v), false, false); return __uint_as_float(r[0]) + __uint_as_float(r[1]); }
__device__ __forceinline__ float x32_sum(float v) { auto r = __builtin_amdgcn_permlane32_swap(__float_as_uint(v), __float_as_uint(v), false, false); return __uint_as_float(r[0]) + __uint_as_float(r[1]); }
__device__ __forceinline__ float x16_max(float v) { auto r = __builtin_amdgcn_permlane16_swap(__float_as_uint(v), __float_as_uint(v), false, false); return fmaxf(__uint_as_float(r[0]), __uint_as_float(r[1])); }
__device__ __forceinline__ float x32_max(float v) { auto r = __builtin_amdgcn_permlane32_swap(__float_as_uint(v), __float_as_uint(v), false, false); return fmaxf(__uint_as_float(r[0]), __uint_as_float(r[1])); }

template <class T> __device__ __forceinline__ T lds_ld(u32 a) { return *(const LAS T*)(size_t)a; }
template <class T> __device__ __forceinline__ void lds_st(u32 a, T v) { *(LAS T*)(size_t)a = v; }
__device__ __forceinline__ v4s lds_tr(u32 a) { return __builtin_amdgcn_ds_read_tr16_b64_v4i16((LAS v4s*)(size_t)a); }
__device__ __forceinline__ v8b cat8(v4s a, v4s b) { return __builtin_bit_cast(v8b, __builtin_shufflevector(a, b, 0, 1, 2, 3, 4, 5, 6, 7)); }
__device__ __forceinline__ v4f mfma32(v8b a, v8b b, v4f c) { return __builtin_amdgcn_mfma_f32_16x16x32_bf16(a, b, c, 0, 0, 0); }

__device__ __forceinline__ void lgkm0() { asm volatile("s_waitcnt lgkmcnt(0)" ::: "memory"); }
template <int N> __device__ __forceinline__ void vmwait() { asm volatile("s_waitcnt vmcnt(%0)" ::"n"(N) : "memory"); }
__device__ __forceinline__ void cbar() { asm volatile("" ::: "memory"); }
__device__ __forceinline__ void wg_barrier() { lgkm0(); __builtin_amdgcn_s_barrier(); cbar(); }

template <int AUX> __device__ __forceinline__ void dma(rsrc_t r, u32 voff, u32 soff, u32 lds) {
  if constexpr (AUX == 1) asm volatile("s_mov_b32 m0, %0\n\ts_nop 0\n\tbuffer_load_dwordx4 %1, %2, %3 offen nt lds" ::"s"(lds), "v"(voff), "s"(r), "s"(soff) : "memory");
  else if constexpr (AUX == 16) asm volatile("s_mov_b32 m0, %0\n\ts_nop 0\n\tbuffer_load_dwordx4 %1, %2, %3 offen sc1 lds" ::"s"(lds), "v"(voff), "s"(r), "s"(soff) : "memory");
  else asm volatile("s_mov_b32 m0, %0\n\ts_nop 0\n\tbuffer_load_dwordx4 %1, %2, %3 offen lds" ::"s"(lds), "v"(voff), "s"(r), "s"(soff) : "memory");
}

__device__ __forceinline__ void set_flag(u32 a, int lane) {
  if (lane == 0) *(volatile LAS u32*)(size_t)a = 1u;
}
__device__ __forceinline__ void wait_flag4(u32 a) {
#pragma unroll 1
  for (u32 it = 0; it < (1u << 22); ++it) {
    v4u v;
    asm volatile("ds_read_b128 %0, %1\n\ts_waitcnt lgkmcnt(0)" : "=v"(v) : "v"(a) : "memory");
    if (__builtin_amdgcn_readfirstlane(v.x & v.y & v.z & v.w)) break;
    __builtin_amdgcn_s_sleep(1);
  }
}
__device__ __forceinline__ void wait_cnt(u32 a, u32 n) {
#pragma unroll 1
  for (u32 it = 0; it < (1u << 22); ++it) {
    u32 v;
    asm volatile("ds_read_b32 %0, %1\n\ts_waitcnt lgkmcnt(0)" : "=v"(v) : "v"(a) : "memory");
    if (__builtin_amdgcn_readfirstlane(v) >= n) break;
    __builtin_amdgcn_s_sleep(1);
  }
}

__device__ __forceinline__ v4u gld(rsrc_t r, u32 off) { return __builtin_amdgcn_raw_buffer_load_b128(r, off, 0, 16); }
__device__ __forceinline__ void gst(rsrc_t r, u32 off, v4u v) { __builtin_amdgcn_raw_buffer_store_b128(v, r, off, 0, 16); }
__device__ __forceinline__ v4u pack4(v4f v, u32 t) {
  v4u r;
#pragma unroll
  for (int i = 0; i < 4; ++i) r[i] = ((__float_as_uint(v[i]) + 0x200u) & ~0x3ffu) | t;
  return r;
}
__device__ __forceinline__ v4f unpack4(v4u w, u32 t, u32& bad) {
  v4u y = w ^ t;
  bad |= y.x | y.y | y.z | y.w;
  return __builtin_bit_cast(v4f, y);
}
__device__ __forceinline__ bool all_ok(u32 bad) { return __all((bad & 0x3ffu) == 0u); }

#define STAMP(i) do { if (i == 0) st0 = (u32)__builtin_amdgcn_s_memrealtime(); if (i == AMP) st1 = (u32)__builtin_amdgcn_s_memrealtime(); } while (0)
#define STAMP_DECL u32 st0 = 0, st1 = 0
#define STAMP_FLUSH() do { } while (0)
#define AMP_SPIN() do { u32 dd_ = ((u32)tb_ - (u32)ta_) >> 20; if (AMP >= 100) { const rsrc_t rs_ = __builtin_amdgcn_make_buffer_rsrc(a.ws, 0, 1 << 20, 0x00020000); u32 mn_ = ~0u, mx_ = 0; \
  for (int k_ = 0; k_ < 4; ++k_) { v2u g_ = __builtin_amdgcn_raw_buffer_load_b64(rs_, WS_END + (k_ * 64 + lane) * 8, 0, 16); u32 rel_ = g_.x - st0 + 100000u; if (g_.y != tag32) rel_ = 100000u; mn_ = min(mn_, rel_); mx_ = max(mx_, rel_); } \
  for (int o_ = 1; o_ < 64; o_ <<= 1) { mn_ = min(mn_, (u32)__shfl_xor((int)mn_, o_)); mx_ = max(mx_, (u32)__shfl_xor((int)mx_, o_)); } \
  const u32 e_ = (u32)__builtin_amdgcn_s_memrealtime() - st0 + 100000u; \
  dd_ = AMP == 100 ? 100000u - mn_ : (AMP == 101 ? mx_ - mn_ : e_ - mn_); dd_ = __builtin_amdgcn_readfirstlane(dd_); } \
  const u32 d_ = dd_ * 10u; const u32 n_ = (u32)__builtin_amdgcn_s_memrealtime(); \
  for (u32 it_ = 0; it_ < (1u << 20); ++it_) { if ((u32)__builtin_amdgcn_s_memrealtime() - n_ >= d_ || d_ > 100000u) break; __builtin_amdgcn_s_sleep(2); } } while (0)

__device__ __forceinline__ u32 t128_tr(int lane, int t, int nb) {
  const int g = lane >> 4, q = (lane & 15) >> 2, p = lane & 3;
  const int r = 8 * g + 4 * t + q;
  const int f = (q >> 1) | (((t ^ g) & 1) << 1);
  return (u32)(r * 128 + ((nb ^ f) << 5) + ((p >> 1) << 4) + ((p & 1) << 3));
}
__device__ __forceinline__ u32 off_b(u32 row, u32 ch) { return 256u * row + 16u * (ch ^ (((row & 3) << 2) | ((row >> 2) & 3))); }

template <int NP> __device__ __forceinline__ v4f gather_rows(rsrc_t rws, u32 base, int lane, u32 t) {
  const int pq = lane >> 4, q = lane & 15;
  const u32 o = base + pq * 2304 + q * 16;
  v4f s;
#pragma unroll 1
  for (u32 it = 0;; ++it) {
    cbar();
    u32 bad = 0;
    v4u w0 = gld(rws, o), w1 = gld(rws, o + 4 * 2304), w2;
    if constexpr (NP == 9) w2 = gld(rws, base + 8 * 2304 + q * 16);
    s = unpack4(w0, t, bad) + unpack4(w1, t, bad);
    if constexpr (NP == 9) { v4f e = unpack4(w2, t, bad); if (pq == 0) s += e; }
    if (all_ok(bad) || it >= SPIN_MAX) break;
  }
#pragma unroll
  for (int i = 0; i < 4; ++i) s[i] = x32_sum(x16_sum(s[i]));
  return s;
}

__device__ __forceinline__ v4f pick4(v4f a0, v4f a1, v4f a2, v4f a3, int c16) {
  const bool b0 = c16 & 1, b1 = c16 & 2;
  v4f r;
#pragma unroll
  for (int i = 0; i < 4; ++i) {
    const float lo = b0 ? a1[i] : a0[i], hi = b0 ? a3[i] : a2[i];
    r[i] = b1 ? hi : lo;
  }
  return r;
}

__global__ void __launch_bounds__(768, 3) fused_decode_kernel(Args a_unused) {
  unsigned long long ta_, tb_;
  asm volatile("s_memrealtime %0\n\ts_waitcnt lgkmcnt(0)" : "=s"(ta_) :: "memory");
  typedef __attribute__((address_space(4))) const Args* cargs_t;
  cargs_t ap_ = (cargs_t)__builtin_amdgcn_kernarg_segment_ptr();
  asm volatile("" : "+s"(ap_) :: "memory");
  Args a; a.x = ap_->x; a.kc = ap_->kc; a.vc = ap_->vc; a.wq = ap_->wq; a.wo = ap_->wo; a.w1 = ap_->w1; a.w2 = ap_->w2; a.out = ap_->out; a.ws = ap_->ws;
  asm volatile("s_waitcnt lgkmcnt(0)\n\ts_memrealtime %0\n\ts_waitcnt lgkmcnt(0)" : "=s"(tb_) : "s"(a.x), "s"(a.ws) : "memory");
  extern __shared__ __attribute__((aligned(16))) unsigned char smem[];
  const int tid = threadIdx.x, lane = tid & 63;
  const int wv = __builtin_amdgcn_readfirstlane(tid >> 6);
  const int b = blockIdx.x, x = b & 7, j = b >> 3;
  const int g = 2 * x + (j >> 4), s = j & 15;
  const int role = j < 9 ? 0 : (j < 18 ? 1 : (j < 27 ? 2 : 3));
  const int jj = j - 9 * role;
  const rsrc_t rws = __builtin_amdgcn_make_buffer_rsrc(a.ws, 0, WS_END, 0x00020000);
  STAMP_DECL;
  STAMP(0);
  int cw;
  u32 tag32;

  if (wv < 4) {
    const int lw = wv;
    constexpr int W = WIN;
    constexpr int N_WQ = 5, N_K = 13, N_V1 = 13 + NV1, N_QG = N_V1 + 2, N_V2 = 23;
    auto after = [&](auto n_) {
      constexpr int n = decltype(n_)::value;
      if constexpr (n > W) vmwait<W>();
      if constexpr (n - W == N_WQ) set_flag(F_WQ + 4 * lw, lane);
      if constexpr (n - W == N_QG) set_flag(F_Q + 4 * lw, lane);
      if constexpr (n - W == N_V2) set_flag(F_V + 4 * lw, lane);
    };
    const u32 vt = (u32)(((lane & 7) ^ (((lane >> 4) & 1) << 1) ^ (((lane >> 5) & 1) << 2)) * 16);
    {
      const rsrc_t rq = __builtin_amdgcn_make_buffer_rsrc((void*)a.wq, 0, 576 * 4096 * 2, 0x00020000);
      const u32 voff = (u32)(lane >> 3) * 8192u + vt;
      const u32 base = (u32)(s >> 2) * 144u * 8192u + (u32)(g * 256 + (s & 3) * 64) * 2u;
      static_for<5>([&](auto k_) {
        constexpr int k = decltype(k_)::value;
        const int i = min(lw + 4 * k, 17);
        dma<BULK_AUX>(rq, voff ^ (u32)((i & 1) << 6), base + i * 65536u, L_WQ + i * 1024u);
        after(std::integral_constant<int, k + 1>{});
      });
    }
    __builtin_amdgcn_s_barrier();
    cbar();
    const u32 vkv = ((u32)(lane >> 4) * 256u + (u32)(((lane & 15) ^ ((lane >> 4) << 2)) * 16)) ^ (u32)(lw << 4);
    const u32 bkv = (u32)(g * 8192 + 6144 + s * 128) * 256u + lw * 1024u;
    const rsrc_t rk = __builtin_amdgcn_make_buffer_rsrc((void*)a.kc, 0, 16 * 8192 * 256, 0x00020000);
    const rsrc_t rv = __builtin_amdgcn_make_buffer_rsrc((void*)a.vc, 0, 16 * 8192 * 256, 0x00020000);
    static_for<8>([&](auto k_) {
      constexpr int k = decltype(k_)::value;
      dma<BULK_AUX>(rk, vkv, bkv + k * 4096u, L_K + lw * 1024u + k * 4096u);
      after(std::integral_constant<int, N_WQ + k + 1>{});
    });
    static_for<NV1>([&](auto k_) {
      constexpr int k = decltype(k_)::value;
      dma<BULK_AUX>(rv, vkv, bkv + k * 4096u, L_V + lw * 1024u + k * 4096u);
      after(std::integral_constant<int, N_K + k + 1>{});
    });
    vmwait<NV1>();
    if constexpr (N_V1 - W < N_WQ) set_flag(F_WQ + 4 * lw, lane);
    STAMP(1);
    wait_flag4(F_QD);
    static_for<2>([&](auto k_) {
      constexpr int k = decltype(k_)::value;
      dma<16>(rws, (u32)lane * 16u, WS_QP + g * 8192u + (lw + 4 * k) * 1024u, L_QST + (lw + 4 * k) * 1024u);
      after(std::integral_constant<int, N_V1 + k + 1>{});
    });
    static_for<8 - NV1>([&](auto k_) {
      constexpr int k = decltype(k_)::value;
      dma<BULK_AUX>(rv, vkv, bkv + (NV1 + k) * 4096u, L_V + lw * 1024u + (NV1 + k) * 4096u);
      after(std::integral_constant<int, N_QG + k + 1>{});
    });
    if (role < 3) {
      const void* wp = role == 0 ? a.wo : (role == 1 ? a.w1 : a.w2);
      const u32 wbytes = role == 0 ? 4096u * 576u * 2u : 4608u * 576u * 2u;
      const rsrc_t rw = __builtin_amdgcn_make_buffer_rsrc((void*)wp, 0, wbytes, 0x00020000);
      const u32 rs = role == 1 ? 9216u : 1152u;
      const u32 base = role == 0 ? (u32)x * 512u * 1152u + (u32)jj * 128u
                     : role == 1 ? (u32)jj * 64u * 9216u + (u32)x * 1152u
                                 : (u32)x * 576u * 1152u + (u32)jj * 128u;
      const int nk = role == 0 ? 16 : 18;
      const u32 voff = (u32)(lane >> 3) * rs + vt;
      static_for<18>([&](auto k_) {
        constexpr int k = decltype(k_)::value;
        if (k < nk) {
          const u32 i = lw + 4 * k;
          const u32 rb = role == 1 ? (i * 7282u) >> 16 : i;
          const u32 p = role == 1 ? i - rb * 9u : 0u;
          dma<BULK_AUX>(rw, voff ^ ((rb & 1u) << 6), base + rb * 8u * rs + p * 128u, L_S + (p * 8u + rb) * 1024u);
          after(std::integral_constant<int, N_V2 + k + 1>{});
        }
      });
      static_assert(N_V2 + W <= N_V2 + 16, "flags must be inside the guarded range");
    } else {
      if constexpr (N_V2 - W < N_QG) { vmwait<8 - NV1>(); set_flag(F_Q + 4 * lw, lane); }
      vmwait<0>();
      set_flag(F_V + 4 * lw, lane);
    }
    vmwait<0>();
    set_flag(F_S + 4 * lw, lane);
    STAMP(2);
    if (lw != 0 || role == 0 || role == 3) { STAMP_FLUSH(); return; }
    lgkm0();
    tag32 = lds_ld<u32>(F_TAG);
    cw = 8;
  } else {
    cw = wv - 4;
    const u32 E = __builtin_amdgcn_raw_buffer_load_b32(rws, WS_EP, 0, 16);
    v4u xa, xb;
    if (cw < 4) {
      const rsrc_t rx = __builtin_amdgcn_make_buffer_rsrc((void*)a.x, 0, 1152, 0x00020000);
      xa = __builtin_amdgcn_raw_buffer_load_b128(rx, lane * 16, 0, 0);
      xb = __builtin_amdgcn_raw_buffer_load_b128(rx, 1024 + lane * 16, 0, 0);
    }
    if (cw == 7 && lane < 32) lds_st<u32>(L_FL + lane * 4, 0u);
    wg_barrier();
    tag32 = __builtin_amdgcn_readfirstlane(E) + 1u;
    if (AMP >= 100 && cw == 0 && lane == 0) __builtin_amdgcn_raw_buffer_store_b64((v2u){st0, tag32}, __builtin_amdgcn_make_buffer_rsrc(a.ws, 0, 1 << 20, 0x00020000), WS_END + b * 8, 0, 16);
    const int g16 = lane >> 4, c16 = lane & 15;
    if (cw < 4) {
      float v[16];
#pragma unroll
      for (int i = 0; i < 4; ++i) { v[2 * i] = bf_lo(xa[i]); v[2 * i + 1] = bf_hi(xa[i]); v[8 + 2 * i] = bf_lo(xb[i]); v[9 + 2 * i] = bf_hi(xb[i]); }
      float sm = 0.f;
#pragma unroll
      for (int i = 0; i < 16; ++i) sm += v[i];
      sm = x32_sum(x16_sum(r16_sum(sm)));
      const float mean = sm * (1.0f / 576.0f);
      float s1 = 0.f, s2 = 0.f;
#pragma unroll
      for (int i = 0; i < 8; ++i) { const float d = v[i] - mean, e = v[8 + i] - mean; s1 += d * d; s2 += e * e; }
      if (lane < 8) s1 += s2;
      s1 = x32_sum(x16_sum(r16_sum(s1)));
      const float m = rbf(mean);
      const float den = rbf(__builtin_amdgcn_sqrtf(rbf(rbf(s1 * (1.0f / 576.0f)) + 1e-5f)));
      const float rden = __builtin_amdgcn_rcpf(den);
      v4u na, nb;
#pragma unroll
      for (int i = 0; i < 4; ++i) {
        const u32 d0 = pk_bf(v[2 * i] - m, v[2 * i + 1] - m), d1 = pk_bf(v[8 + 2 * i] - m, v[9 + 2 * i] - m);
        na[i] = pk_bf(bf_lo(d0) * rden, bf_hi(d0) * rden);
        nb[i] = pk_bf(bf_lo(d1) * rden, bf_hi(d1) * rden);
      }
      lds_st<v4u>(L_XN + lane * 16, na);
      lds_st<v4u>(L_XR + lane * 16, xa);
      if (lane < 8) { lds_st<v4u>(L_XN + 1024 + lane * 16, nb); lds_st<v4u>(L_XR + 1024 + lane * 16, xb); }
      if (cw == 0 && lane == 0) lds_st<u32>(F_TAG, tag32);
      lgkm0();
      STAMP(1);
      wait_flag4(F_WQ);
      STAMP(2);
      const int ks = s >> 2;
      const u32 a0 = L_WQ + t128_tr(lane, 0, cw), a1 = L_WQ + t128_tr(lane, 1, cw);
      v4f acc = {0.f, 0.f, 0.f, 0.f};
#pragma unroll
      for (int c = 0; c < 4; ++c) {
        const v8b A = cat8(lds_tr(a0 + c * 4096), lds_tr(a1 + c * 4096));
        const v8b B = __builtin_bit_cast(v8b, lds_ld<v4u>(L_XN + (ks * 18 + 4 * c + g16) * 16));
        acc = mfma32(A, B, acc);
      }
      {
        const int q = (lane & 15) >> 2, p = lane & 3, r = 128 + 4 * g16 + q;
        const int f = (q >> 1) | (((g16 ^ (g16 >> 1)) & 1) << 1);
        const v4s A = lds_tr(L_WQ + r * 128 + ((cw ^ f) << 5) + ((p >> 1) << 4) + ((p & 1) << 3));
        const v4s B = __builtin_bit_cast(v4s, lds_ld<v2u>(L_XN + (ks * 144 + 128 + 4 * g16) * 2));
        acc = __builtin_amdgcn_mfma_f32_16x16x16bf16_1k(A, B, acc, 0, 0, 0);
      }
      if (c16 < 2) {
        const float v0 = c16 ? acc[2] : acc[0], v1 = c16 ? acc[3] : acc[1];
        gst(rws, WS_QP + g * 8192u + (u32)(ks * 128 + (s & 3) * 32 + cw * 8 + 2 * g16 + c16) * 16u,
            (v4u){__float_as_uint(v0), tag32, __float_as_uint(v1), tag32});
      }
      lgkm0();
      set_flag(F_QD + 4 * cw, lane);
      STAMP(3);
    }
    const u32 t10 = tag32 & 0x3ffu;
    wait_flag4(F_Q);
    STAMP(4);
    {
      float q0[2] = {0.f, 0.f}, q1[2] = {0.f, 0.f};
      u32 bad = 0;
#pragma unroll
      for (int k = 0; k < 4; ++k) {
        const v4u u0 = lds_ld<v4u>(L_QST + (k * 128 + lane) * 16), u1 = lds_ld<v4u>(L_QST + (k * 128 + 64 + lane) * 16);
        q0[0] += __uint_as_float(u0.x); q0[1] += __uint_as_float(u0.z);
        q1[0] += __uint_as_float(u1.x); q1[1] += __uint_as_float(u1.z);
        bad |= (u0.y ^ tag32) | (u0.w ^ tag32) | (u1.y ^ tag32) | (u1.w ^ tag32);
      }
      if (!__all(bad == 0u)) {
#pragma unroll 1
        for (u32 it = 0; it < SPIN_MAX; ++it) {
          cbar();
          bad = 0; q0[0] = q0[1] = q1[0] = q1[1] = 0.f;
#pragma unroll
          for (int k = 0; k < 4; ++k) {
            const v4u u0 = gld(rws, WS_QP + g * 8192u + (k * 128 + lane) * 16), u1 = gld(rws, WS_QP + g * 8192u + (k * 128 + 64 + lane) * 16);
            q0[0] += __uint_as_float(u0.x); q0[1] += __uint_as_float(u0.z);
            q1[0] += __uint_as_float(u1.x); q1[1] += __uint_as_float(u1.z);
            bad |= (u0.y ^ tag32) | (u0.w ^ tag32) | (u1.y ^ tag32) | (u1.w ^ tag32);
          }
          if (__all(bad == 0u)) break;
        }
        STAMP(15);
      }
      lds_st<u32>(L_QV + cw * 576 + lane * 4, pk_bf(q0[0], q0[1]));
      lds_st<u32>(L_QV + cw * 576 + 272 + lane * 4, pk_bf(q1[0], q1[1]));
    }
    {
      const u32 key = 16 * cw + c16;
      v4f acc = {0.f, 0.f, 0.f, 0.f};
#pragma unroll
      for (int c = 0; c < 4; ++c) {
        const v8b A = __builtin_bit_cast(v8b, lds_ld<v4u>(L_K + off_b(key, 4 * c + g16)));
        const v8b B = __builtin_bit_cast(v8b, lds_ld<v4u>(L_QV + cw * 576 + (lane & 1) * 272 + (32 * c + 8 * g16) * 2));
        acc = mfma32(A, B, acc);
      }
      v4f sc;
#pragma unroll
      for (int i = 0; i < 4; ++i) sc[i] = rbf(rbf(acc[i]) * 0.08838834764831845f);
      if (c16 < 2) lds_st<v4f>(L_SC + c16 * 512 + (16 * cw + 4 * g16) * 4, sc);
      lgkm0();
      if (lane == 0) __hip_atomic_fetch_add((LAS u32*)(size_t)F_SC, 1u, __ATOMIC_RELAXED, __HIP_MEMORY_SCOPE_WORKGROUP);
      wait_cnt(F_SC, 8);
    }
    STAMP(5);
    {
      const v4f sc = lds_ld<v4f>(L_SC + (lane >> 5) * 512 + (lane & 31) * 16);
      float m = fmaxf(fmaxf(sc[0], sc[1]), fmaxf(sc[2], sc[3]));
      m = x16_max(r16_max(m));
      v4f e;
#pragma unroll
      for (int i = 0; i < 4; ++i) e[i] = __builtin_amdgcn_exp2f((sc[i] - m) * 1.4426950408889634f);
      float l = (e[0] + e[1]) + (e[2] + e[3]);
      l = x16_sum(r16_sum(l));
      lds_st<v2u>(L_P + cw * 576 + (lane >> 5) * 272 + (lane & 31) * 8, (v2u){pk_bf(e[0], e[1]), pk_bf(e[2], e[3])});
      if (cw == 0) {
        const float m1 = __uint_as_float(__builtin_amdgcn_readlane(__float_as_uint(m), 32)), l1 = __uint_as_float(__builtin_amdgcn_readlane(__float_as_uint(l), 32));
        if (lane == 0) gst(rws, WS_OP + (g * 16 + s) * OPS + 1024, pack4((v4f){m, l, m1, l1}, t10));
      }
    }
    v8b PB[4];
#pragma unroll
    for (int c = 0; c < 4; ++c) PB[c] = __builtin_bit_cast(v8b, lds_ld<v4u>(L_P + cw * 576 + (lane & 1) * 272 + (32 * c + 8 * g16) * 2));
    wait_flag4(F_V);
    STAMP(6);
    {
      const int q = (lane & 15) >> 2, p = lane & 3;
      v4f acc = {0.f, 0.f, 0.f, 0.f};
#pragma unroll
      for (int c = 0; c < 4; ++c) {
        const u32 r0 = 32 * c + 8 * g16 + q;
        const v4s lo = lds_tr(L_V + off_b(r0, 2 * cw + (p >> 1)) + 8 * (p & 1));
        const v4s hi = lds_tr(L_V + off_b(r0 + 4, 2 * cw + (p >> 1)) + 8 * (p & 1));
        acc = mfma32(cat8(lo, hi), PB[c], acc);
      }
      if (c16 < 2) gst(rws, WS_OP + (g * 16 + s) * OPS + c16 * 512 + (16 * cw + 4 * g16) * 4, pack4(acc, t10));
    }
    STAMP(7);
    if (role == 3) {
      const int r = (j - 27) * 8 + x;
      if (r >= 9 || cw != 0) { STAMP_FLUSH(); return; }
      const v4f hs = gather_rows<8>(rws, WS_HP + r * 256, lane, t10);
      const v2u xr = lds_ld<v2u>(L_XR + (64 * r + 4 * c16) * 2);
      float h[4];
      h[0] = rbf(bf_lo(xr.x) + rbf(hs[0])); h[1] = rbf(bf_hi(xr.x) + rbf(hs[1]));
      h[2] = rbf(bf_lo(xr.y) + rbf(hs[2])); h[3] = rbf(bf_hi(xr.y) + rbf(hs[3]));
      STAMP(8);
      const v4f fs = gather_rows<8>(rws, WS_FP + r * 256, lane, t10);
      STAMP(9);
      if (lane < 16) {
        const v2u o = {pk_bf(h[0] + rbf(fs[0]), h[1] + rbf(fs[1])), pk_bf(h[2] + rbf(fs[2]), h[3] + rbf(fs[3]))};
        *(v2u*)((char*)a.out + (64 * r + 4 * c16) * 2) = o;
      }
      if (r == 0 && lane == 0) __builtin_amdgcn_raw_buffer_store_b32(tag32, rws, WS_EP, 0, 16);
      STAMP(10);
      STAMP_FLUSH();
      if (r == 0) AMP_SPIN();
      return;
    }
  }

  const u32 t10 = tag32 & 0x3ffu;
  const int g16 = lane >> 4, c16 = lane & 15;
  wait_flag4(F_S);
  STAMP(8);
  v8b A[4][2];
  {
    const u32 sb = L_S + cw * 8192;
#pragma unroll
    for (int nb = 0; nb < 4; ++nb)
#pragma unroll
      for (int c = 0; c < 2; ++c)
        A[nb][c] = cat8(lds_tr(sb + t128_tr(lane, 0, nb) + c * 4096), lds_tr(sb + t128_tr(lane, 1, nb) + c * 4096));
  }
  u32 vec;
  if (role == 0) {
    const int gg = 2 * x + (cw >> 2), hh = (cw >> 1) & 1;
    const u32 ob = WS_OP + (gg * 16 + 4 * g16) * OPS;
    v4f o[4], st[4];
#pragma unroll 1
    for (u32 it = 0;; ++it) {
      cbar();
      u32 bad = 0;
      v4u w[4], u[4];
#pragma unroll
      for (int i = 0; i < 4; ++i) { w[i] = gld(rws, ob + i * OPS + hh * 512 + (cw & 1) * 256 + c16 * 16); u[i] = gld(rws, ob + i * OPS + 1024); }
#pragma unroll
      for (int i = 0; i < 4; ++i) { o[i] = unpack4(w[i], t10, bad); st[i] = unpack4(u[i], t10, bad); }
      if (all_ok(bad) || it >= SPIN_MAX) break;
    }
    STAMP(9);
    float m[4], l[4];
#pragma unroll
    for (int i = 0; i < 4; ++i) { m[i] = hh ? st[i][2] : st[i][0]; l[i] = hh ? st[i][3] : st[i][1]; }
    float M = fmaxf(fmaxf(m[0], m[1]), fmaxf(m[2], m[3]));
    M = x32_max(x16_max(M));
    float L = 0.f;
    v4f acc = {0.f, 0.f, 0.f, 0.f};
#pragma unroll
    for (int i = 0; i < 4; ++i) {
      const float e = __builtin_amdgcn_exp2f((m[i] - M) * 1.4426950408889634f);
      L += e * l[i];
      acc += e * o[i];
    }
    L = x32_sum(x16_sum(L));
    const float rl = __builtin_amdgcn_rcpf(L);
#pragma unroll
    for (int i = 0; i < 4; ++i) acc[i] = x32_sum(x16_sum(acc[i])) * rl;
    vec = L_VEC + cw * 128;
    if (lane < 16) lds_st<v2u>(vec + c16 * 8, (v2u){pk_bf(acc[0], acc[1]), pk_bf(acc[2], acc[3])});
  } else if (role == 1) {
    const v4f hs = gather_rows<8>(rws, WS_HP + cw * 256, lane, t10);
    STAMP(9);
    const v2u xr = lds_ld<v2u>(L_XR + (64 * cw + 4 * c16) * 2);
    const u32 h01 = pk_bf(bf_lo(xr.x) + rbf(hs[0]), bf_hi(xr.x) + rbf(hs[1]));
    const u32 h23 = pk_bf(bf_lo(xr.y) + rbf(hs[2]), bf_hi(xr.y) + rbf(hs[3]));
    const float h0 = bf_lo(h01), h1 = bf_hi(h01), h2 = bf_lo(h23), h3 = bf_hi(h23);
    const float mw = r16_sum((h0 + h1) + (h2 + h3)) * (1.0f / 64.0f);
    const float d0 = h0 - mw, d1 = h1 - mw, d2 = h2 - mw, d3 = h3 - mw;
    const float m2 = r16_sum((d0 * d0 + d1 * d1) + (d2 * d2 + d3 * d3));
    if (lane < 16) lds_st<v2u>(L_H + (64 * cw + 4 * c16) * 2, (v2u){h01, h23});
    if (lane == 0) lds_st<v2f>(L_ST + cw * 8, (v2f){mw, m2});
    wg_barrier();
    const v2f sw = lds_ld<v2f>(L_ST + (c16 < 9 ? c16 : 0) * 8);
    const u32 hv = lds_ld<unsigned short>(L_H + (64 * jj + lane) * 2);
    const float mean = r16_sum(c16 < 9 ? sw[0] : 0.f) * (1.0f / 9.0f);
    const float dm = sw[0] - mean;
    const float var = r16_sum(c16 < 9 ? sw[1] + 64.0f * dm * dm : 0.f) * (1.0f / 576.0f);
    const float den = rbf(__builtin_amdgcn_sqrtf(rbf(rbf(var) + 1e-5f)));
    const float xn = rbf(bf_lo(hv) - rbf(mean)) * __builtin_amdgcn_rcpf(den);
    vec = L_XN2 + cw * 128;
    lds_st<unsigned short>(vec + lane * 2, (unsigned short)pk_bf(xn, xn));
  } else {
    const v4f ps = gather_rows<9>(rws, WS_PP + x * 9 * 2304 + cw * 256, lane, t10);
    STAMP(9);
    float gv[4];
#pragma unroll
    for (int i = 0; i < 4; ++i) {
      const float pre = rbf(ps[i]);
      gv[i] = pre * __builtin_amdgcn_rcpf(1.0f + __builtin_amdgcn_exp2f(pre * -1.4426950408889634f));
    }
    vec = L_VEC + cw * 128;
    if (lane < 16) lds_st<v2u>(vec + c16 * 8, (v2u){pk_bf(gv[0], gv[1]), pk_bf(gv[2], gv[3])});
  }
  v4f r;
  {
    const v8b B0 = __builtin_bit_cast(v8b, lds_ld<v4u>(vec + g16 * 16)), B1 = __builtin_bit_cast(v8b, lds_ld<v4u>(vec + 64 + g16 * 16));
    const v4f z = {0.f, 0.f, 0.f, 0.f};
    const v4f c0 = mfma32(A[0][1], B1, mfma32(A[0][0], B0, z)), c1 = mfma32(A[1][1], B1, mfma32(A[1][0], B0, z));
    const v4f c2 = mfma32(A[2][1], B1, mfma32(A[2][0], B0, z)), c3 = mfma32(A[3][1], B1, mfma32(A[3][0], B0, z));
    r = pick4(c0, c1, c2, c3, c16);
  }
  if (role == 1) {
    if (c16 < 4) gst(rws, WS_PP + (x * 9 + jj) * 2304 + cw * 256 + (16 * c16 + 4 * g16) * 4, pack4(r, t10));
    STAMP(10);
    STAMP_FLUSH();
    return;
  }
  if (c16 < 4) lds_st<v4f>(L_PART + cw * 256 + (16 * c16 + 4 * g16) * 4, r);
  wg_barrier();
  if (cw == 0) {
    v4f t = lds_ld<v4f>(L_PART + g16 * 256 + c16 * 16) + lds_ld<v4f>(L_PART + (g16 + 4) * 256 + c16 * 16);
    if (role == 2) { const v4f e = lds_ld<v4f>(L_PART + 8 * 256 + c16 * 16); if (g16 == 0) t += e; }
#pragma unroll
    for (int i = 0; i < 4; ++i) t[i] = x32_sum(x16_sum(t[i]));
    if (lane < 16) gst(rws, (role == 0 ? WS_HP : WS_FP) + x * 2304 + jj * 256 + c16 * 16, pack4(t, t10));
  }
  STAMP(10);
  STAMP_FLUSH();
}

}

extern "C" void kernel_launch(void* const* d_in, const int* in_sizes, int n_in, void* d_out, int out_size, void* d_ws, size_t ws_size,
                              hipStream_t stream) {
  (void)in_sizes; (void)n_in; (void)out_size; (void)ws_size;
  Args a;
  a.x = d_in[0]; a.kc = d_in[1]; a.vc = d_in[2]; a.wq = d_in[3]; a.wo = d_in[4]; a.w1 = d_in[6]; a.w2 = d_in[7];
  a.out = d_out; a.ws = d_ws;
  (void)hipFuncSetAttribute((const void*)fused_decode_kernel, hipFuncAttributeMaxDynamicSharedMemorySize, (int)L_END);
  hipLaunchKernelGGL(fused_decode_kernel, dim3(256), dim3(768), L_END, stream, a);
}
```

```cpp
#include <hip/hip_runtime.h>
#include <stdint.h>
#include <type_traits>

namespace {

#define LAS __attribute__((address_space(3)))
typedef unsigned int u32;
typedef u32 v4u __attribute__((ext_vector_type(4)));
typedef u32 v2u __attribute__((ext_vector_type(2)));
typedef float v4f __attribute__((ext_vector_type(4)));
typedef float v2f __attribute__((ext_vector_type(2)));
typedef short v4s __attribute__((ext_vector_type(4)));
typedef short v8s __attribute__((ext_vector_type(8)));
typedef __bf16 v8b __attribute__((ext_vector_type(8)));
typedef __bf16 v2b __attribute__((ext_vector_type(2)));
typedef __amdgpu_buffer_rsrc_t rsrc_t;

#define WIN 10
#ifndef NV1
#define NV1 4
#endif
#define BULK_AUX 1

constexpr u32 L_WQ = 0, L_K = 18432, L_V = 51200, L_S = 83968, L_FL = 157696;
constexpr u32 L_XN = L_FL + 128, L_SC = L_XN + 1152, L_Y = L_SC + 1024, L_END = 163840;
constexpr u32 L_QST = 0, L_QV = 8192, L_P = L_QV + 8 * 576;
constexpr u32 F_WQ = L_FL, F_Q = L_FL + 16, F_V = L_FL + 32, F_S = L_FL + 48, F_QD = L_FL + 64, F_SC = L_FL + 80, F_TAG = L_FL + 96;
constexpr u32 L_XR = L_Y, L_ST = L_Y + 1152, L_H = L_ST + 128, L_XN2 = L_H + 1152;
constexpr u32 L_VEC = L_Y, L_PART = L_Y + 1152;
static_assert(L_XN2 + 9 * 128 <= L_END && L_PART + 9 * 256 <= L_END, "lds");
constexpr u32 WS_EP = 0, WS_QP = 4096, WS_OP = WS_QP + 16 * 8192, OPS = 1152, WS_HP = WS_OP + 256 * OPS;
constexpr u32 WS_PP = WS_HP + 8 * 2304, WS_FP = WS_PP + 72 * 2304, WS_END = WS_FP + 8 * 2304;
constexpr u32 SPIN_MAX = 1u << 16;

struct Args {
  const void* x; const void* kc; const void* vc; const void* wq; const void* wo; const void* w1; const void* w2;
  void* out; void* ws;
};

template <int N, class F> __device__ __forceinline__ void static_for(F&& f) {
  if constexpr (N > 0) { static_for<N - 1>(f); f(std::integral_constant<int, N - 1>{}); }
}

__device__ __forceinline__ float bf_lo(u32 u) { return __uint_as_float(u << 16); }
__device__ __forceinline__ float bf_hi(u32 u) { return __uint_as_float(u & 0xffff0000u); }
__device__ __forceinline__ u32 pk_bf(float lo, float hi) {
  v2b r = __builtin_convertvector((v2f){lo, hi}, v2b);
  return __builtin_bit_cast(u32, r);
}
__device__ __forceinline__ float rbf(float f) { return bf_lo(pk_bf(f, f)); }

template <int C> __device__ __forceinline__ float dppf(float v) {
  return __builtin_bit_cast(float, __builtin_amdgcn_update_dpp(0, __builtin_bit_cast(int, v), C, 0xf, 0xf, true));
}
__device__ __forceinline__ float r16_sum(float v) { v += dppf<0xB1>(v); v += dppf<0x4E>(v); v += dppf<0x141>(v); v += dppf<0x140>(v); return v; }
__device__ __forceinline__ float r16_max(float v) { v = fmaxf(v, dppf<0xB1>(v)); v = fmaxf(v, dppf<0x4E>(v)); v = fmaxf(v, dppf<0x141>(v)); v = fmaxf(v, dppf<0x140>(v)); return v; }
__device__ __forceinline__ float x16_sum(float v) { auto r = __builtin_amdgcn_permlane16_swap(__float_as_uint(v), __float_as_uint(v), false, false); return __uint_as_float(r[0]) + __uint_as_float(r[1]); }
__device__ __forceinline__ float x32_sum(float v) { auto r = __builtin_amdgcn_permlane32_swap(__float_as_uint(v), __float_as_uint(v), false, false); return __uint_as_float(r[0]) + __uint_as_float(r[1]); }
__device__ __forceinline__ float x16_max(float v) { auto r = __builtin_amdgcn_permlane16_swap(__float_as_uint(v), __float_as_uint(v), false, false); return fmaxf(__uint_as_float(r[0]), __uint_as_float(r[1])); }
__device__ __forceinline__ float x32_max(float v) { auto r = __builtin_amdgcn_permlane32_swap(__float_as_uint(v), __float_as_uint(v), false, false); return fmaxf(__uint_as_float(r[0]), __uint_as_float(r[1])); }

template <class T> __device__ __forceinline__ T lds_ld(u32 a) { return *(const LAS T*)(size_t)a; }
template <class T> __device__ __forceinline__ void lds_st(u32 a, T v) { *(LAS T*)(size_t)a = v; }
__device__ __forceinline__ v4s lds_tr(u32 a) { return __builtin_amdgcn_ds_read_tr16_b64_v4i16((LAS v4s*)(size_t)a); }
__device__ __forceinline__ v8b cat8(v4s a, v4s b) { return __builtin_bit_cast(v8b, __builtin_shufflevector(a, b, 0, 1, 2, 3, 4, 5, 6, 7)); }
__device__ __forceinline__ v4f mfma32(v8b a, v8b b, v4f c) { return __builtin_amdgcn_mfma_f32_16x16x32_bf16(a, b, c, 0, 0, 0); }

__device__ __forceinline__ void lgkm0() { asm volatile("s_waitcnt lgkmcnt(0)" ::: "memory"); }
template <int N> __device__ __forceinline__ void vmwait() { asm volatile("s_waitcnt vmcnt(%0)" ::"n"(N) : "memory"); }
__device__ __forceinline__ void cbar() { asm volatile("" ::: "memory"); }
__device__ __forceinline__ void wg_barrier() { lgkm0(); __builtin_amdgcn_s_barrier(); cbar(); }

template <int AUX> __device__ __forceinline__ void dma(rsrc_t r, u32 voff, u32 soff, u32 lds) {
  if constexpr (AUX == 1) asm volatile("s_mov_b32 m0, %0\n\ts_nop 0\n\tbuffer_load_dwordx4 %1, %2, %3 offen nt lds" ::"s"(lds), "v"(voff), "s"(r), "s"(soff) : "memory");
  else if constexpr (AUX == 16) asm volatile("s_mov_b32 m0, %0\n\ts_nop 0\n\tbuffer_load_dwordx4 %1, %2, %3 offen sc1 lds" ::"s"(lds), "v"(voff), "s"(r), "s"(soff) : "memory");
  else asm volatile("s_mov_b32 m0, %0\n\ts_nop 0\n\tbuffer_load_dwordx4 %1, %2, %3 offen lds" ::"s"(lds), "v"(voff), "s"(r), "s"(soff) : "memory");
}

__device__ __forceinline__ void set_flag(u32 a, int lane) {
  if (lane == 0) *(volatile LAS u32*)(size_t)a = 1u;
}
__device__ __forceinline__ void wait_flag4(u32 a) {
#pragma unroll 1
  for (u32 it = 0; it < (1u << 22); ++it) {
    v4u v;
    asm volatile("ds_read_b128 %0, %1\n\ts_waitcnt lgkmcnt(0)" : "=v"(v) : "v"(a) : "memory");
    if (__builtin_amdgcn_readfirstlane(v.x & v.y & v.z & v.w)) break;
    __builtin_amdgcn_s_sleep(1);
  }
}
__device__ __forceinline__ void wait_cnt(u32 a, u32 n) {
#pragma unroll 1
  for (u32 it = 0; it < (1u << 22); ++it) {
    u32 v;
    asm volatile("ds_read_b32 %0, %1\n\ts_waitcnt lgkmcnt(0)" : "=v"(v) : "v"(a) : "memory");
    if (__builtin_amdgcn_readfirstlane(v) >= n) break;
    __builtin_amdgcn_s_sleep(1);
  }
}

__device__ __forceinline__ v4u gld(rsrc_t r, u32 off) { return __builtin_amdgcn_raw_buffer_load_b128(r, off, 0, 16); }
__device__ __forceinline__ void gst(rsrc_t r, u32 off, v4u v) { __builtin_amdgcn_raw_buffer_store_b128(v, r, off, 0, 16); }
__device__ __forceinline__ v4u pack4(v4f v, u32 t) {
  v4u r;
#pragma unroll
  for (int i = 0; i < 4; ++i) r[i] = ((__float_as_uint(v[i]) + 0x200u) & ~0x3ffu) | t;
  return r;
}
__device__ __forceinline__ v4f unpack4(v4u w, u32 t, u32& bad) {
  v4u y = w ^ t;
  bad |= y.x | y.y | y.z | y.w;
  return __builtin_bit_cast(v4f, y);
}
__device__ __forceinline__ bool all_ok(u32 bad) { return __all((bad & 0x3ffu) == 0u); }

#define STAMP(i) do { } while (0)
#define STAMP_DECL do { } while (0)
#define STAMP_FLUSH() do { } while (0)
#define AMP_SPIN() do { } while (0)

__device__ __forceinline__ u32 t128_tr(int lane, int t, int nb) {
  const int g = lane >> 4, q = (lane & 15) >> 2, p = lane & 3;
  const int r = 8 * g + 4 * t + q;
  const int f = (q >> 1) | (((t ^ g) & 1) << 1);
  return (u32)(r * 128 + ((nb ^ f) << 5) + ((p >> 1) << 4) + ((p & 1) << 3));
}
__device__ __forceinline__ u32 off_b(u32 row, u32 ch) { return 256u * row + 16u * (ch ^ (((row & 3) << 2) | ((row >> 2) & 3))); }

template <int NP> __device__ __forceinline__ v4f gather_rows(rsrc_t rws, u32 base, int lane, u32 t) {
  const int pq = lane >> 4, q = lane & 15;
  const u32 o = base + pq * 2304 + q * 16;
  v4f s;
#pragma unroll 1
  for (u32 it = 0;; ++it) {
    cbar();
    u32 bad = 0;
    v4u w0 = gld(rws, o), w1 = gld(rws, o + 4 * 2304), w2;
    if constexpr (NP == 9) w2 = gld(rws, base + 8 * 2304 + q * 16);
    s = unpack4(w0, t, bad) + unpack4(w1, t, bad);
    if constexpr (NP == 9) { v4f e = unpack4(w2, t, bad); if (pq == 0) s += e; }
    if (all_ok(bad) || it >= SPIN_MAX) break;
  }
#pragma unroll
  for (int i = 0; i < 4; ++i) s[i] = x32_sum(x16_sum(s[i]));
  return s;
}

__device__ __forceinline__ v4f pick4(v4f a0, v4f a1, v4f a2, v4f a3, int c16) {
  const bool b0 = c16 & 1, b1 = c16 & 2;
  v4f r;
#pragma unroll
  for (int i = 0; i < 4; ++i) {
    const float lo = b0 ? a1[i] : a0[i], hi = b0 ? a3[i] : a2[i];
    r[i] = b1 ? hi : lo;
  }
  return r;
}

__global__ void __launch_bounds__(768, 3) fused_decode_kernel(Args a) {
  extern __shared__ __attribute__((aligned(16))) unsigned char smem[];
  const int tid = threadIdx.x, lane = tid & 63;
  const int wv = __builtin_amdgcn_readfirstlane(tid >> 6);
  const int b = blockIdx.x, x = b & 7, j = b >> 3;
  const int g = 2 * x + (j >> 4), s = j & 15;
  const int role = j < 9 ? 0 : (j < 18 ? 1 : (j < 27 ? 2 : 3));
  const int jj = j - 9 * role;
  const rsrc_t rws = __builtin_amdgcn_make_buffer_rsrc(a.ws, 0, WS_END, 0x00020000);
  STAMP_DECL;
  STAMP(0);
  int cw;
  u32 tag32;

  if (wv < 4) {
    const int lw = wv;
    constexpr int W = WIN;
    constexpr int N_WQ = 5, N_K = 13, N_V1 = 13 + NV1, N_QG = N_V1 + 2, N_V2 = 23;
    auto after = [&](auto n_) {
      constexpr int n = decltype(n_)::value;
      if constexpr (n > W) vmwait<W>();
      if constexpr (n - W == N_WQ) set_flag(F_WQ + 4 * lw, lane);
      if constexpr (n - W == N_QG) set_flag(F_Q + 4 * lw, lane);
      if constexpr (n - W == N_V2) set_flag(F_V + 4 * lw, lane);
    };
    const u32 vt = (u32)(((lane & 7) ^ (((lane >> 4) & 1) << 1) ^ (((lane >> 5) & 1) << 2)) * 16);
    {
      const rsrc_t rq = __builtin_amdgcn_make_buffer_rsrc((void*)a.wq, 0, 576 * 4096 * 2, 0x00020000);
      const u32 voff = (u32)(lane >> 3) * 8192u + vt;
      const u32 base = (u32)(s >> 2) * 144u * 8192u + (u32)(g * 256 + (s & 3) * 64) * 2u;
      static_for<5>([&](auto k_) {
        constexpr int k = decltype(k_)::value;
        const int i = min(lw + 4 * k, 17);
        dma<BULK_AUX>(rq, voff ^ (u32)((i & 1) << 6), base + i * 65536u, L_WQ + i * 1024u);
        after(std::integral_constant<int, k + 1>{});
      });
    }
    __builtin_amdgcn_s_barrier();
    cbar();
    const u32 vkv = ((u32)(lane >> 4) * 256u + (u32)(((lane & 15) ^ ((lane >> 4) << 2)) * 16)) ^ (u32)(lw << 4);
    const u32 bkv = (u32)(g * 8192 + 6144 + s * 128) * 256u + lw * 1024u;
    const rsrc_t rk = __builtin_amdgcn_make_buffer_rsrc((void*)a.kc, 0, 16 * 8192 * 256, 0x00020000);
    const rsrc_t rv = __builtin_amdgcn_make_buffer_rsrc((void*)a.vc, 0, 16 * 8192 * 256, 0x00020000);
    static_for<8>([&](auto k_) {
      constexpr int k = decltype(k_)::value;
      dma<BULK_AUX>(rk, vkv, bkv + k * 4096u, L_K + lw * 1024u + k * 4096u);
      after(std::integral_constant<int, N_WQ + k + 1>{});
    });
    static_for<NV1>([&](auto k_) {
      constexpr int k = decltype(k_)::value;
      dma<BULK_AUX>(rv, vkv, bkv + k * 4096u, L_V + lw * 1024u + k * 4096u);
      after(std::integral_constant<int, N_K + k + 1>{});
    });
    vmwait<NV1>();
    if constexpr (N_V1 - W < N_WQ) set_flag(F_WQ + 4 * lw, lane);
    STAMP(1);
    wait_flag4(F_QD);
    static_for<2>([&](auto k_) {
      constexpr int k = decltype(k_)::value;
      dma<16>(rws, (u32)lane * 16u, WS_QP + g * 8192u + (lw + 4 * k) * 1024u, L_QST + (lw + 4 * k) * 1024u);
      after(std::integral_constant<int, N_V1 + k + 1>{});
    });
    static_for<8 - NV1>([&](auto k_) {
      constexpr int k = decltype(k_)::value;
      dma<BULK_AUX>(rv, vkv, bkv + (NV1 + k) * 4096u, L_V + lw * 1024u + (NV1 + k) * 4096u);
      after(std::integral_constant<int, N_QG + k + 1>{});
    });
    if (role < 3) {
      const void* wp = role == 0 ? a.wo : (role == 1 ? a.w1 : a.w2);
      const u32 wbytes = role == 0 ? 4096u * 576u * 2u : 4608u * 576u * 2u;
      const rsrc_t rw = __builtin_amdgcn_make_buffer_rsrc((void*)wp, 0, wbytes, 0x00020000);
      const u32 rs = role == 1 ? 9216u : 1152u;
      const u32 base = role == 0 ? (u32)x * 512u * 1152u + (u32)jj * 128u
                     : role == 1 ? (u32)jj * 64u * 9216u + (u32)x * 1152u
                                 : (u32)x * 576u * 1152u + (u32)jj * 128u;
      const int nk = role == 0 ? 16 : 18;
      const u32 voff = (u32)(lane >> 3) * rs + vt;
      static_for<18>([&](auto k_) {
        constexpr int k = decltype(k_)::value;
        if (k < nk) {
          const u32 i = lw + 4 * k;
          const u32 rb = role == 1 ? (i * 7282u) >> 16 : i;
          const u32 p = role == 1 ? i - rb * 9u : 0u;
          dma<BULK_AUX>(rw, voff ^ ((rb & 1u) << 6), base + rb * 8u * rs + p * 128u, L_S + (p * 8u + rb) * 1024u);
          after(std::integral_constant<int, N_V2 + k + 1>{});
        }
      });
      static_assert(N_V2 + W <= N_V2 + 16, "flags must be inside the guarded range");
    } else {
      if constexpr (N_V2 - W < N_QG) { vmwait<8 - NV1>(); set_flag(F_Q + 4 * lw, lane); }
      vmwait<0>();
      set_flag(F_V + 4 * lw, lane);
    }
    vmwait<0>();
    set_flag(F_S + 4 * lw, lane);
    STAMP(2);
    if (lw != 0 || role == 0 || role == 3) { STAMP_FLUSH(); return; }
    lgkm0();
    tag32 = lds_ld<u32>(F_TAG);
    cw = 8;
  } else {
    cw = wv - 4;
    const u32 E = __builtin_amdgcn_raw_buffer_load_b32(rws, WS_EP, 0, 16);
    v4u xa, xb;
    if (cw < 4) {
      const rsrc_t rx = __builtin_amdgcn_make_buffer_rsrc((void*)a.x, 0, 1152, 0x00020000);
      xa = __builtin_amdgcn_raw_buffer_load_b128(rx, lane * 16, 0, 0);
      xb = __builtin_amdgcn_raw_buffer_load_b128(rx, 1024 + lane * 16, 0, 0);
    }
    if (cw == 7 && lane < 32) lds_st<u32>(L_FL + lane * 4, 0u);
    wg_barrier();
    tag32 = __builtin_amdgcn_readfirstlane(E) + 1u;
    const int g16 = lane >> 4, c16 = lane & 15;
    if (cw < 4) {
      float v[16];
#pragma unroll
      for (int i = 0; i < 4; ++i) { v[2 * i] = bf_lo(xa[i]); v[2 * i + 1] = bf_hi(xa[i]); v[8 + 2 * i] = bf_lo(xb[i]); v[9 + 2 * i] = bf_hi(xb[i]); }
      float sm = 0.f;
#pragma unroll
      for (int i = 0; i < 16; ++i) sm += v[i];
      sm = x32_sum(x16_sum(r16_sum(sm)));
      const float mean = sm * (1.0f / 576.0f);
      float s1 = 0.f, s2 = 0.f;
#pragma unroll
      for (int i = 0; i < 8; ++i) { const float d = v[i] - mean, e = v[8 + i] - mean; s1 += d * d; s2 += e * e; }
      if (lane < 8) s1 += s2;
      s1 = x32_sum(x16_sum(r16_sum(s1)));
      const float m = rbf(mean);
      const float den = rbf(__builtin_amdgcn_sqrtf(rbf(rbf(s1 * (1.0f / 576.0f)) + 1e-5f)));
      const float rden = __builtin_amdgcn_rcpf(den);
      v4u na, nb;
#pragma unroll
      for (int i = 0; i < 4; ++i) {
        const u32 d0 = pk_bf(v[2 * i] - m, v[2 * i + 1] - m), d1 = pk_bf(v[8 + 2 * i] - m, v[9 + 2 * i] - m);
        na[i] = pk_bf(bf_lo(d0) * rden, bf_hi(d0) * rden);
        nb[i] = pk_bf(bf_lo(d1) * rden, bf_hi(d1) * rden);
      }
      lds_st<v4u>(L_XN + lane * 16, na);
      lds_st<v4u>(L_XR + lane * 16, xa);
      if (lane < 8) { lds_st<v4u>(L_XN + 1024 + lane * 16, nb); lds_st<v4u>(L_XR + 1024 + lane * 16, xb); }
      if (cw == 0 && lane == 0) lds_st<u32>(F_TAG, tag32);
      lgkm0();
      STAMP(1);
      wait_flag4(F_WQ);
      STAMP(2);
      const int ks = s >> 2;
      const u32 a0 = L_WQ + t128_tr(lane, 0, cw), a1 = L_WQ + t128_tr(lane, 1, cw);
      v4f acc = {0.f, 0.f, 0.f, 0.f};
#pragma unroll
      for (int c = 0; c < 4; ++c) {
        const v8b A = cat8(lds_tr(a0 + c * 4096), lds_tr(a1 + c * 4096));
        const v8b B = __builtin_bit_cast(v8b, lds_ld<v4u>(L_XN + (ks * 18 + 4 * c + g16) * 16));
        acc = mfma32(A, B, acc);
      }
      {
        const int q = (lane & 15) >> 2, p = lane & 3, r = 128 + 4 * g16 + q;
        const int f = (q >> 1) | (((g16 ^ (g16 >> 1)) & 1) << 1);
        const v4s A = lds_tr(L_WQ + r * 128 + ((cw ^ f) << 5) + ((p >> 1) << 4) + ((p & 1) << 3));
        const v4s B = __builtin_bit_cast(v4s, lds_ld<v2u>(L_XN + (ks * 144 + 128 + 4 * g16) * 2));
        acc = __builtin_amdgcn_mfma_f32_16x16x16bf16_1k(A, B, acc, 0, 0, 0);
      }
      if (c16 < 2) {
        const float v0 = c16 ? acc[2] : acc[0], v1 = c16 ? acc[3] : acc[1];
        gst(rws, WS_QP + g * 8192u + (u32)(ks * 128 + (s & 3) * 32 + cw * 8 + 2 * g16 + c16) * 16u,
            (v4u){__float_as_uint(v0), tag32, __float_as_uint(v1), tag32});
      }
      lgkm0();
      set_flag(F_QD + 4 * cw, lane);
      STAMP(3);
    }
    const u32 t10 = tag32 & 0x3ffu;
    wait_flag4(F_Q);
    STAMP(4);
    {
      float q0[2] = {0.f, 0.f}, q1[2] = {0.f, 0.f};
      u32 bad = 0;
#pragma unroll
      for (int k = 0; k < 4; ++k) {
        const v4u u0 = lds_ld<v4u>(L_QST + (k * 128 + lane) * 16), u1 = lds_ld<v4u>(L_QST + (k * 128 + 64 + lane) * 16);
        q0[0] += __uint_as_float(u0.x); q0[1] += __uint_as_float(u0.z);
        q1[0] += __uint_as_float(u1.x); q1[1] += __uint_as_float(u1.z);
        bad |= (u0.y ^ tag32) | (u0.w ^ tag32) | (u1.y ^ tag32) | (u1.w ^ tag32);
      }
      if (!__all(bad == 0u)) {
#pragma unroll 1
        for (u32 it = 0; it < SPIN_MAX; ++it) {
          cbar();
          bad = 0; q0[0] = q0[1] = q1[0] = q1[1] = 0.f;
#pragma unroll
          for (int k = 0; k < 4; ++k) {
            const v4u u0 = gld(rws, WS_QP + g * 8192u + (k * 128 + lane) * 16), u1 = gld(rws, WS_QP + g * 8192u + (k * 128 + 64 + lane) * 16);
            q0[0] += __uint_as_float(u0.x); q0[1] += __uint_as_float(u0.z);
            q1[0] += __uint_as_float(u1.x); q1[1] += __uint_as_float(u1.z);
            bad |= (u0.y ^ tag32) | (u0.w ^ tag32) | (u1.y ^ tag32) | (u1.w ^ tag32);
          }
          if (__all(bad == 0u)) break;
        }
        STAMP(15);
      }
      lds_st<u32>(L_QV + cw * 576 + lane * 4, pk_bf(q0[0], q0[1]));
      lds_st<u32>(L_QV + cw * 576 + 272 + lane * 4, pk_bf(q1[0], q1[1]));
    }
    {
      const u32 key = 16 * cw + c16;
      v4f acc = {0.f, 0.f, 0.f, 0.f};
#pragma unroll
      for (int c = 0; c < 4; ++c) {
        const v8b A = __builtin_bit_cast(v8b, lds_ld<v4u>(L_K + off_b(key, 4 * c + g16)));
        const v8b B = __builtin_bit_cast(v8b, lds_ld<v4u>(L_QV + cw * 576 + (lane & 1) * 272 + (32 * c + 8 * g16) * 2));
        acc = mfma32(A, B, acc);
      }
      v4f sc;
#pragma unroll
      for (int i = 0; i < 4; ++i) sc[i] = rbf(rbf(acc[i]) * 0.08838834764831845f);
      if (c16 < 2) lds_st<v4f>(L_SC + c16 * 512 + (16 * cw + 4 * g16) * 4, sc);
      lgkm0();
      if (lane == 0) __hip_atomic_fetch_add((LAS u32*)(size_t)F_SC, 1u, __ATOMIC_RELAXED, __HIP_MEMORY_SCOPE_WORKGROUP);
      wait_cnt(F_SC, 8);
    }
    STAMP(5);
    {
      const v4f sc = lds_ld<v4f>(L_SC + (lane >> 5) * 512 + (lane & 31) * 16);
      float m = fmaxf(fmaxf(sc[0], sc[1]), fmaxf(sc[2], sc[3]));
      m = x16_max(r16_max(m));
      v4f e;
#pragma unroll
      for (int i = 0; i < 4; ++i) e[i] = __builtin_amdgcn_exp2f((sc[i] - m) * 1.4426950408889634f);
      float l = (e[0] + e[1]) + (e[2] + e[3]);
      l = x16_sum(r16_sum(l));
      lds_st<v2u>(L_P + cw * 576 + (lane >> 5) * 272 + (lane & 31) * 8, (v2u){pk_bf(e[0], e[1]), pk_bf(e[2], e[3])});
      if (cw == 0) {
        const float m1 = __uint_as_float(__builtin_amdgcn_readlane(__float_as_uint(m), 32)), l1 = __uint_as_float(__builtin_amdgcn_readlane(__float_as_uint(l), 32));
        if (lane == 0) gst(rws, WS_OP + (g * 16 + s) * OPS + 1024, pack4((v4f){m, l, m1, l1}, t10));
      }
    }
    v8b PB[4];
#pragma unroll
    for (int c = 0; c < 4; ++c) PB[c] = __builtin_bit_cast(v8b, lds_ld<v4u>(L_P + cw * 576 + (lane & 1) * 272 + (32 * c + 8 * g16) * 2));
    wait_flag4(F_V);
    STAMP(6);
    {
      const int q = (lane & 15) >> 2, p = lane & 3;
      v4f acc = {0.f, 0.f, 0.f, 0.f};
#pragma unroll
      for (int c = 0; c < 4; ++c) {
        const u32 r0 = 32 * c + 8 * g16 + q;
        const v4s lo = lds_tr(L_V + off_b(r0, 2 * cw + (p >> 1)) + 8 * (p & 1));
        const v4s hi = lds_tr(L_V + off_b(r0 + 4, 2 * cw + (p >> 1)) + 8 * (p & 1));
        acc = mfma32(cat8(lo, hi), PB[c], acc);
      }
      if (c16 < 2) gst(rws, WS_OP + (g * 16 + s) * OPS + c16 * 512 + (16 * cw + 4 * g16) * 4, pack4(acc, t10));
    }
    STAMP(7);
    if (role == 3) {
      const int r = (j - 27) * 8 + x;
      if (r >= 9 || cw != 0) { STAMP_FLUSH(); return; }
      const v4f hs = gather_rows<8>(rws, WS_HP + r * 256, lane, t10);
      const v2u xr = lds_ld<v2u>(L_XR + (64 * r + 4 * c16) * 2);
      float h[4];
      h[0] = rbf(bf_lo(xr.x) + rbf(hs[0])); h[1] = rbf(bf_hi(xr.x) + rbf(hs[1]));
      h[2] = rbf(bf_lo(xr.y) + rbf(hs[2])); h[3] = rbf(bf_hi(xr.y) + rbf(hs[3]));
      STAMP(8);
      const v4f fs = gather_rows<8>(rws, WS_FP + r * 256, lane, t10);
      STAMP(9);
      if (lane < 16) {
        const v2u o = {pk_bf(h[0] + rbf(fs[0]), h[1] + rbf(fs[1])), pk_bf(h[2] + rbf(fs[2]), h[3] + rbf(fs[3]))};
        *(v2u*)((char*)a.out + (64 * r + 4 * c16) * 2) = o;
      }
      if (r == 0 && lane == 0) __builtin_amdgcn_raw_buffer_store_b32(tag32, rws, WS_EP, 0, 16);
      STAMP(10);
      STAMP_FLUSH();
      if (r == 0) AMP_SPIN();
      return;
    }
  }

  const u32 t10 = tag32 & 0x3ffu;
  const int g16 = lane >> 4, c16 = lane & 15;
  wait_flag4(F_S);
  STAMP(8);
  v8b A[4][2];
  {
    const u32 sb = L_S + cw * 8192;
#pragma unroll
    for (int nb = 0; nb < 4; ++nb)
#pragma unroll
      for (int c = 0; c < 2; ++c)
        A[nb][c] = cat8(lds_tr(sb + t128_tr(lane, 0, nb) + c * 4096), lds_tr(sb + t128_tr(lane, 1, nb) + c * 4096));
  }
  u32 vec;
  if (role == 0) {
    const int gg = 2 * x + (cw >> 2), hh = (cw >> 1) & 1;
    const u32 ob = WS_OP + (gg * 16 + 4 * g16) * OPS;
    v4f o[4], st[4];
#pragma unroll 1
    for (u32 it = 0;; ++it) {
      cbar();
      u32 bad = 0;
      v4u w[4], u[4];
#pragma unroll
      for (int i = 0; i < 4; ++i) { w[i] = gld(rws, ob + i * OPS + hh * 512 + (cw & 1) * 256 + c16 * 16); u[i] = gld(rws, ob + i * OPS + 1024); }
#pragma unroll
      for (int i = 0; i < 4; ++i) { o[i] = unpack4(w[i], t10, bad); st[i] = unpack4(u[i], t10, bad); }
      if (all_ok(bad) || it >= SPIN_MAX) break;
    }
    STAMP(9);
    float m[4], l[4];
#pragma unroll
    for (int i = 0; i < 4; ++i) { m[i] = hh ? st[i][2] : st[i][0]; l[i] = hh ? st[i][3] : st[i][1]; }
    float M = fmaxf(fmaxf(m[0], m[1]), fmaxf(m[2], m[3]));
    M = x32_max(x16_max(M));
    float L = 0.f;
    v4f acc = {0.f, 0.f, 0.f, 0.f};
#pragma unroll
    for (int i = 0; i < 4; ++i) {
      const float e = __builtin_amdgcn_exp2f((m[i] - M) * 1.4426950408889634f);
      L += e * l[i];
      acc += e * o[i];
    }
    L = x32_sum(x16_sum(L));
    const float rl = __builtin_amdgcn_rcpf(L);
#pragma unroll
    for (int i = 0; i < 4; ++i) acc[i] = x32_sum(x16_sum(acc[i])) * rl;
    vec = L_VEC + cw * 128;
    if (lane < 16) lds_st<v2u>(vec + c16 * 8, (v2u){pk_bf(acc[0], acc[1]), pk_bf(acc[2], acc[3])});
  } else if (role == 1) {
    const v4f hs = gather_rows<8>(rws, WS_HP + cw * 256, lane, t10);
    STAMP(9);
    const v2u xr = lds_ld<v2u>(L_XR + (64 * cw + 4 * c16) * 2);
    const u32 h01 = pk_bf(bf_lo(xr.x) + rbf(hs[0]), bf_hi(xr.x) + rbf(hs[1]));
    const u32 h23 = pk_bf(bf_lo(xr.y) + rbf(hs[2]), bf_hi(xr.y) + rbf(hs[3]));
    const float h0 = bf_lo(h01), h1 = bf_hi(h01), h2 = bf_lo(h23), h3 = bf_hi(h23);
    const float mw = r16_sum((h0 + h1) + (h2 + h3)) * (1.0f / 64.0f);
    const float d0 = h0 - mw, d1 = h1 - mw, d2 = h2 - mw, d3 = h3 - mw;
    const float m2 = r16_sum((d0 * d0 + d1 * d1) + (d2 * d2 + d3 * d3));
    if (lane < 16) lds_st<v2u>(L_H + (64 * cw + 4 * c16) * 2, (v2u){h01, h23});
    if (lane == 0) lds_st<v2f>(L_ST + cw * 8, (v2f){mw, m2});
    wg_barrier();
    const v2f sw = lds_ld<v2f>(L_ST + (c16 < 9 ? c16 : 0) * 8);
    const u32 hv = lds_ld<unsigned short>(L_H + (64 * jj + lane) * 2);
    const float mean = r16_sum(c16 < 9 ? sw[0] : 0.f) * (1.0f / 9.0f);
    const float dm = sw[0] - mean;
    const float var = r16_sum(c16 < 9 ? sw[1] + 64.0f * dm * dm : 0.f) * (1.0f / 576.0f);
    const float den = rbf(__builtin_amdgcn_sqrtf(rbf(rbf(var) + 1e-5f)));
    const float xn = rbf(bf_lo(hv) - rbf(mean)) * __builtin_amdgcn_rcpf(den);
    vec = L_XN2 + cw * 128;
    lds_st<unsigned short>(vec + lane * 2, (unsigned short)pk_bf(xn, xn));
  } else {
    const v4f ps = gather_rows<9>(rws, WS_PP + x * 9 * 2304 + cw * 256, lane, t10);
    STAMP(9);
    float gv[4];
#pragma unroll
    for (int i = 0; i < 4; ++i) {
      const float pre = rbf(ps[i]);
      gv[i] = pre * __builtin_amdgcn_rcpf(1.0f + __builtin_amdgcn_exp2f(pre * -1.4426950408889634f));
    }
    vec = L_VEC + cw * 128;
    if (lane < 16) lds_st<v2u>(vec + c16 * 8, (v2u){pk_bf(gv[0], gv[1]), pk_bf(gv[2], gv[3])});
  }
  v4f r;
  {
    const v8b B0 = __builtin_bit_cast(v8b, lds_ld<v4u>(vec + g16 * 16)), B1 = __builtin_bit_cast(v8b, lds_ld<v4u>(vec + 64 + g16 * 16));
    const v4f z = {0.f, 0.f, 0.f, 0.f};
    const v4f c0 = mfma32(A[0][1], B1, mfma32(A[0][0], B0, z)), c1 = mfma32(A[1][1], B1, mfma32(A[1][0], B0, z));
    const v4f c2 = mfma32(A[2][1], B1, mfma32(A[2][0], B0, z)), c3 = mfma32(A[3][1], B1, mfma32(A[3][0], B0, z));
    r = pick4(c0, c1, c2, c3, c16);
  }
  if (role == 1) {
    if (c16 < 4) gst(rws, WS_PP + (x * 9 + jj) * 2304 + cw * 256 + (16 * c16 + 4 * g16) * 4, pack4(r, t10));
    STAMP(10);
    STAMP_FLUSH();
    return;
  }
  if (c16 < 4) lds_st<v4f>(L_PART + cw * 256 + (16 * c16 + 4 * g16) * 4, r);
  wg_barrier();
  if (cw == 0) {
    v4f t = lds_ld<v4f>(L_PART + g16 * 256 + c16 * 16) + lds_ld<v4f>(L_PART + (g16 + 4) * 256 + c16 * 16);
    if (role == 2) { const v4f e = lds_ld<v4f>(L_PART + 8 * 256 + c16 * 16); if (g16 == 0) t += e; }
#pragma unroll
    for (int i = 0; i < 4; ++i) t[i] = x32_sum(x16_sum(t[i]));
    if (lane < 16) gst(rws, (role == 0 ? WS_HP : WS_FP) + x * 2304 + jj * 256 + c16 * 16, pack4(t, t10));
  }
  STAMP(10);
  STAMP_FLUSH();
}

__global__ void null_kernel(u32* p) { if (threadIdx.x == 0) p[0] = (u32)__builtin_amdgcn_s_memrealtime(); }
template <int BIGV> __global__ void __launch_bounds__(768, 3) probe_x(u32* p, u32 amp) {
  extern __shared__ unsigned char smem3[];
  const u32 n_ = (u32)__builtin_amdgcn_s_memrealtime();
  if (BIGV) asm volatile("v_mov_b32 v95, 0" ::: "v95");
  if (blockIdx.x == gridDim.x - 1 && threadIdx.x < 64) {
    const u32 d_ = (n_ - __builtin_amdgcn_readfirstlane(*(volatile u32*)p)) * amp;
    for (u32 it_ = 0; it_ < (1u << 20); ++it_) { if ((u32)__builtin_amdgcn_s_memrealtime() - n_ >= d_ || d_ > 100000u) break; __builtin_amdgcn_s_sleep(2); }
  }
}
}

extern "C" void kernel_launch(void* const* d_in, const int* in_sizes, int n_in, void* d_out, int out_size, void* d_ws, size_t ws_size,
                              hipStream_t stream) {
  (void)in_sizes; (void)n_in; (void)out_size; (void)ws_size;
  Args a;
  a.x = d_in[0]; a.kc = d_in[1]; a.vc = d_in[2]; a.wq = d_in[3]; a.wo = d_in[4]; a.w1 = d_in[6]; a.w2 = d_in[7];
  a.out = d_out; a.ws = d_ws;
  (void)hipFuncSetAttribute((const void*)fused_decode_kernel, hipFuncAttributeMaxDynamicSharedMemorySize, (int)L_END);
  u32* pp_ = (u32*)((char*)d_ws + (8 << 20));
  hipLaunchKernelGGL(null_kernel, dim3(1), dim3(64), 0, stream, pp_);
  hipLaunchKernelGGL(probe_x<0>, dim3(1), dim3(64), 0, stream, pp_, 0u);
  hipLaunchKernelGGL(fused_decode_kernel, dim3(256), dim3(768), L_END, stream, a);
}
```

```cpp
#include <hip/hip_runtime.h>
#include <stdint.h>
#include <type_traits>

namespace {

#define LAS __attribute__((address_space(3)))
typedef unsigned int u32;
typedef u32 v4u __attribute__((ext_vector_type(4)));
typedef u32 v2u __attribute__((ext_vector_type(2)));
typedef float v4f __attribute__((ext_vector_type(4)));
typedef float v2f __attribute__((ext_vector_type(2)));
typedef short v4s __attribute__((ext_vector_type(4)));
typedef short v8s __attribute__((ext_vector_type(8)));
typedef __bf16 v8b __attribute__((ext_vector_type(8)));
typedef __bf16 v2b __attribute__((ext_vector_type(2)));
typedef __amdgpu_buffer_rsrc_t rsrc_t;

#define WIN 3
#define NBURST 8
#define WT_W 10
#define WT_M1 6
#define WT_M2 4
#define DLY_M1 0
#define DLY_M2 0
#define BULK_AUX 1
#define WQ_AUX 0
#define PREFETCH 1

constexpr u32 L_WQ = 0, L_K = 18432, L_V = 51200, L_S = 83968, L_FL = 157696;
constexpr u32 L_VEC = L_FL + 128, L_ONE = L_VEC + 1280, L_ST = L_ONE + 1152, L_END = 163840;
constexpr u32 L_Q = 0, L_SC = 1024, L_P = 2048;
constexpr u32 F_WQ = L_FL, F_K = L_FL + 16, F_V1 = L_FL + 32, F_V2 = L_FL + 48, F_S = L_FL + 64, F_Q = L_FL + 80, F_SC = L_FL + 96;
static_assert(L_ST + 128 <= L_END && L_P + 8 * 576 <= L_K, "lds");
constexpr u32 WS_EP = 0, WS_QB = 4096, WS_OP = WS_QB + 16 * 1024, OPS = 1152, WS_HP = WS_OP + 256 * OPS;
constexpr u32 WS_GP = WS_HP + 8 * 2304, WS_FP = WS_GP + 4608 * 4, WS_END = WS_FP + 8 * 2304;
constexpr u32 SPIN_MAX = 1u << 16;

struct Args {
  const void* x; const void* kc; const void* vc; const void* wq; const void* wo; const void* w1; const void* w2;
  void* out; void* ws;
};

template <int N, class F> __device__ __forceinline__ void static_for(F&& f) {
  if constexpr (N > 0) { static_for<N - 1>(f); f(std::integral_constant<int, N - 1>{}); }
}

__device__ __forceinline__ float bf_lo(u32 u) { return __uint_as_float(u << 16); }
__device__ __forceinline__ float bf_hi(u32 u) { return __uint_as_float(u & 0xffff0000u); }
__device__ __forceinline__ u32 pk_bf(float lo, float hi) {
  v2b r = __builtin_convertvector((v2f){lo, hi}, v2b);
  return __builtin_bit_cast(u32, r);
}
__device__ __forceinline__ float rbf(float f) { return bf_lo(pk_bf(f, f)); }

template <int C> __device__ __forceinline__ float dppf(float v) {
  return __builtin_bit_cast(float, __builtin_amdgcn_update_dpp(0, __builtin_bit_cast(int, v), C, 0xf, 0xf, true));
}
__device__ __forceinline__ float r16_sum(float v) { v += dppf<0xB1>(v); v += dppf<0x4E>(v); v += dppf<0x141>(v); v += dppf<0x140>(v); return v; }
__device__ __forceinline__ float r16_max(float v) { v = fmaxf(v, dppf<0xB1>(v)); v = fmaxf(v, dppf<0x4E>(v)); v = fmaxf(v, dppf<0x141>(v)); v = fmaxf(v, dppf<0x140>(v)); return v; }
__device__ __forceinline__ float sw16_add(float a, float b) { auto r = __builtin_amdgcn_permlane16_swap(__float_as_uint(a), __float_as_uint(b), false, false); return __uint_as_float(r[0]) + __uint_as_float(r[1]); }
__device__ __forceinline__ float sw32_add(float a, float b) { auto r = __builtin_amdgcn_permlane32_swap(__float_as_uint(a), __float_as_uint(b), false, false); return __uint_as_float(r[0]) + __uint_as_float(r[1]); }
__device__ __forceinline__ float x16_sum(float v) { return sw16_add(v, v); }
__device__ __forceinline__ float x32_sum(float v) { return sw32_add(v, v); }
__device__ __forceinline__ float x16_max(float v) { auto r = __builtin_amdgcn_permlane16_swap(__float_as_uint(v), __float_as_uint(v), false, false); return fmaxf(__uint_as_float(r[0]), __uint_as_float(r[1])); }
__device__ __forceinline__ float x32_max(float v) { auto r = __builtin_amdgcn_permlane32_swap(__float_as_uint(v), __float_as_uint(v), false, false); return fmaxf(__uint_as_float(r[0]), __uint_as_float(r[1])); }
__device__ __forceinline__ float rs4(v4f a) { return sw32_add(sw16_add(a[0], a[1]), sw16_add(a[2], a[3])); }

template <class T> __device__ __forceinline__ T lds_ld(u32 a) { return *(const LAS T*)(size_t)a; }
template <class T> __device__ __forceinline__ void lds_st(u32 a, T v) { *(LAS T*)(size_t)a = v; }
__device__ __forceinline__ v4s lds_tr(u32 a) { return __builtin_amdgcn_ds_read_tr16_b64_v4i16((LAS v4s*)(size_t)a); }
__device__ __forceinline__ v8b cat8(v4s a, v4s b) { return __builtin_bit_cast(v8b, __builtin_shufflevector(a, b, 0, 1, 2, 3, 4, 5, 6, 7)); }
__device__ __forceinline__ v8b lds_frag(u32 a) { return __builtin_bit_cast(v8b, lds_ld<v4u>(a)); }
__device__ __forceinline__ v4f mfma32(v8b a, v8b b, v4f c) { return __builtin_amdgcn_mfma_f32_16x16x32_bf16(a, b, c, 0, 0, 0); }

__device__ __forceinline__ void lgkm0() { asm volatile("s_waitcnt lgkmcnt(0)" ::: "memory"); }
template <int N> __device__ __forceinline__ void vmwait() { asm volatile("s_waitcnt vmcnt(%0)" ::"n"(N) : "memory"); }
__device__ __forceinline__ void cbar() { asm volatile("" ::: "memory"); }
__device__ __forceinline__ void wg_barrier() { lgkm0(); __builtin_amdgcn_s_barrier(); cbar(); }

template <int AUX> __device__ __forceinline__ void dma(rsrc_t r, u32 voff, u32 soff, u32 lds) {
  if constexpr (AUX == 1) asm volatile("s_mov_b32 m0, %0\n\ts_nop 0\n\tbuffer_load_dwordx4 %1, %2, %3 offen nt lds" ::"s"(lds), "v"(voff), "s"(r), "s"(soff) : "memory");
  else asm volatile("s_mov_b32 m0, %0\n\ts_nop 0\n\tbuffer_load_dwordx4 %1, %2, %3 offen lds" ::"s"(lds), "v"(voff), "s"(r), "s"(soff) : "memory");
}

__device__ __forceinline__ void set_flag(u32 a, int lane) {
  if (lane == 0) *(volatile LAS u32*)(size_t)a = 1u;
}
template <int SLEEP> __device__ __forceinline__ void wait_flag4(u32 a) {
#pragma unroll 1
  for (u32 it = 0; it < (1u << 22); ++it) {
    v4u v;
    asm volatile("ds_read_b128 %0, %1\n\ts_waitcnt lgkmcnt(0)" : "=v"(v) : "v"(a) : "memory");
    if (__builtin_amdgcn_readfirstlane(v.x & v.y & v.z & v.w)) break;
    if (SLEEP) __builtin_amdgcn_s_sleep(SLEEP);
  }
}
template <int SLEEP> __device__ __forceinline__ void wait_cnt(u32 a, u32 n) {
#pragma unroll 1
  for (u32 it = 0; it < (1u << 22); ++it) {
    u32 v;
    asm volatile("ds_read_b32 %0, %1\n\ts_waitcnt lgkmcnt(0)" : "=v"(v) : "v"(a) : "memory");
    if (__builtin_amdgcn_readfirstlane(v) >= n) break;
    if (SLEEP) __builtin_amdgcn_s_sleep(SLEEP);
  }
}

__device__ __forceinline__ v4u gld(rsrc_t r, u32 off) { return __builtin_amdgcn_raw_buffer_load_b128(r, off, 0, 16); }
__device__ __forceinline__ u32 gld1(rsrc_t r, u32 off) { return __builtin_amdgcn_raw_buffer_load_b32(r, off, 0, 16); }
__device__ __forceinline__ void gst(rsrc_t r, u32 off, v4u v) { __builtin_amdgcn_raw_buffer_store_b128(v, r, off, 0, 16); }
__device__ __forceinline__ void gst1(rsrc_t r, u32 off, u32 v) { __builtin_amdgcn_raw_buffer_store_b32(v, r, off, 0, 16); }
__device__ __forceinline__ u32 pack1(float v, u32 t) { return (__float_as_uint(v) & ~0x3ffu) | t; }
__device__ __forceinline__ v4u pack4(v4f v, u32 t) { return (v4u){pack1(v[0], t), pack1(v[1], t), pack1(v[2], t), pack1(v[3], t)}; }
__device__ __forceinline__ bool none(u32 bad) { return __all(bad == 0u); }

template <int N, class Acc> __device__ __forceinline__ void poll4(rsrc_t r, u32 off, u32 stride, u32 t, Acc&& acc) {
  u32 pend = (1u << N) - 1u;
  v4u w[N];
#pragma unroll 1
  for (u32 it = 0; it < SPIN_MAX; ++it) {
    cbar();
    static_for<N>([&](auto i_) { constexpr int i = decltype(i_)::value; if (pend & (1u << i)) w[i] = gld(r, off + i * stride); });
    static_for<N>([&](auto i_) {
      constexpr int i = decltype(i_)::value;
      if (pend & (1u << i)) {
        const v4u y = w[i] ^ t;
        if (none((y.x | y.y | y.z | y.w) & 0x3ffu)) { acc(i_, __builtin_bit_cast(v4f, y)); pend &= ~(1u << i); }
      }
    });
    if (pend == 0u) break;
  }
}

#define STAMP(i) do { } while (0)
#define STAMP_DECL do { } while (0)
#define STAMP_FLUSH() do { } while (0)
#define AMP_SPIN() do { } while (0)

__device__ __forceinline__ u32 t128_tr(int lane, int t, int nb) {
  const int g = lane >> 4, q = (lane & 15) >> 2, p = lane & 3;
  const int r = 8 * g + 4 * t + q;
  const int f = (q >> 1) | (((t ^ g) & 1) << 1);
  return (u32)(r * 128 + ((nb ^ f) << 5) + ((p >> 1) << 4) + ((p & 1) << 3));
}
__device__ __forceinline__ u32 off_b(u32 row, u32 ch) { return 256u * row + 16u * (ch ^ (((row & 3) << 2) | ((row >> 2) & 3))); }

constexpr int N_WQ = 5, N_K = 13, N_V1 = 17, N_V2 = 21;
constexpr int win_at(int n, int wt) { return n <= N_V2 ? WIN : (WIN + (n - N_V2) < wt ? WIN + (n - N_V2) : wt); }
constexpr int landed(int n, int wt) { return n < NBURST ? 0 : (n - win_at(n, wt) > 0 ? n - win_at(n, wt) : 0); }
template <int n, int wt> __device__ __forceinline__ void after(int lw, int lane) {
  if constexpr (n >= NBURST && n > win_at(n, wt)) vmwait<win_at(n, wt)>();
  constexpr int a = landed(n - 1, wt), b = landed(n, wt);
  if constexpr (a < N_WQ && b >= N_WQ) set_flag(F_WQ + 4 * lw, lane);
  if constexpr (a < N_K && b >= N_K) set_flag(F_K + 4 * lw, lane);
  if constexpr (a < N_V1 && b >= N_V1) set_flag(F_V1 + 4 * lw, lane);
  if constexpr (a < N_V2 && b >= N_V2) set_flag(F_V2 + 4 * lw, lane);
}
template <int NK, int WT> __device__ __forceinline__ void tile_loads(rsrc_t rw, u32 voff, u32 base, u32 rs8, int lw, int lane) {
  static_assert(landed(N_V2 + NK, WT) >= N_V2, "V flags must be published inside the tile phase");
  static_for<NK>([&](auto k_) {
    constexpr int k = decltype(k_)::value;
    const u32 i = lw + 4 * k;
    dma<BULK_AUX>(rw, voff ^ ((i & 1u) << 6), base + i * rs8, L_S + i * 1024u);
    after<N_V2 + k + 1, WT>(lw, lane);
  });
}
__device__ __forceinline__ void delay_ticks(u32 n) {
  if (n == 0u) return;
  const u32 t0 = (u32)__builtin_amdgcn_s_memrealtime();
#pragma unroll 1
  for (u32 it = 0; it < 4096u; ++it) { if ((u32)__builtin_amdgcn_s_memrealtime() - t0 >= n) break; __builtin_amdgcn_s_sleep(1); }
}
__device__ __forceinline__ u32 load_epoch(const void* ws) {
  u32 e;
  asm volatile("s_load_dword %0, %1, 0x0\n\ts_waitcnt lgkmcnt(0)" : "=s"(e) : "s"(ws) : "memory");
  return e;
}

__global__ void __launch_bounds__(768, 3) fused_decode_kernel(Args a) {
  extern __shared__ __attribute__((aligned(16))) unsigned char smem[];
  const int tid = threadIdx.x, lane = tid & 63;
  const int wv = __builtin_amdgcn_readfirstlane(tid >> 6);
  const int b = blockIdx.x, x = b & 7, j = b >> 3;
  const int g = 2 * x + (j >> 4), s = j & 15;
  const int role = j < 9 ? 0 : (j < 18 ? 1 : (j < 27 ? 2 : 3));
  const int jj = j - 9 * role;
  const int g16 = lane >> 4, c16 = lane & 15;
  const rsrc_t rws = __builtin_amdgcn_make_buffer_rsrc(a.ws, 0, WS_END, 0x00020000);
  const rsrc_t rx = __builtin_amdgcn_make_buffer_rsrc((void*)a.x, 0, 1152, 0x00020000);
  STAMP_DECL;
  STAMP(0);
  int cw;
  u32 tag32;
  u32 xres = 0;
  v8b B[18];

  if (wv < 4) {
    const int lw = wv;
    const u32 vt = (u32)(((lane & 7) ^ (((lane >> 4) & 1) << 1) ^ (((lane >> 5) & 1) << 2)) * 16);
    {
      const rsrc_t rq = __builtin_amdgcn_make_buffer_rsrc((void*)a.wq, 0, 576 * 4096 * 2, 0x00020000);
      const u32 voff = (u32)(lane >> 1) * 8192u + (u32)(lane & 1) * 16u;
      const u32 base = (u32)(g * 256 + s * 16) * 2u;
      static_for<5>([&](auto k_) {
        constexpr int k = decltype(k_)::value;
        const int i = min(lw + 4 * k, 17);
        dma<WQ_AUX>(rq, voff, base + i * 262144u, L_WQ + i * 1024u);
      });
    }
    static_assert(NBURST > 5, "no flag may be published before the first barrier");
    __builtin_amdgcn_s_barrier();
    cbar();
    const u32 vkv = ((u32)(lane >> 4) * 256u + (u32)(((lane & 15) ^ ((lane >> 4) << 2)) * 16)) ^ (u32)(lw << 4);
    const u32 bkv = (u32)(g * 8192 + 6144 + s * 128) * 256u + lw * 1024u;
    const rsrc_t rk = __builtin_amdgcn_make_buffer_rsrc((void*)a.kc, 0, 16 * 8192 * 256, 0x00020000);
    const rsrc_t rv = __builtin_amdgcn_make_buffer_rsrc((void*)a.vc, 0, 16 * 8192 * 256, 0x00020000);
    static_for<8>([&](auto k_) {
      constexpr int k = decltype(k_)::value;
      dma<BULK_AUX>(rk, vkv, bkv + k * 4096u, L_K + lw * 1024u + k * 4096u);
      after<N_WQ + k + 1, WIN>(lw, lane);
    });
    STAMP(1);
    tag32 = load_epoch(a.ws) + 1u;
    static_for<8>([&](auto k_) {
      constexpr int k = decltype(k_)::value;
      dma<BULK_AUX>(rv, vkv, bkv + k * 4096u, L_V + lw * 1024u + k * 4096u);
      after<N_K + k + 1, WIN>(lw, lane);
    });
    STAMP(2);
    if (role == 3) {
      static_assert(landed(N_V2, WIN) >= N_K, "");
      if constexpr (landed(N_V2, WIN) < N_V1) { vmwait<4>(); set_flag(F_V1 + 4 * lw, lane); }
      vmwait<0>();
      set_flag(F_V2 + 4 * lw, lane);
      STAMP_FLUSH();
      return;
    }
    {
      const void* wp = role == 0 ? a.wo : (role == 1 ? a.w1 : a.w2);
      const u32 wbytes = role == 0 ? 4096u * 576u * 2u : 4608u * 576u * 2u;
      const rsrc_t rw = __builtin_amdgcn_make_buffer_rsrc((void*)wp, 0, wbytes, 0x00020000);
      const u32 rs = role == 1 ? 9216u : 1152u;
      const u32 base = role == 0 ? (u32)x * 512u * 1152u + (u32)jj * 128u
                     : role == 1 ? (u32)(x * 9 + jj) * 128u
                                 : (u32)x * 576u * 1152u + (u32)jj * 128u;
      const u32 voff = (u32)(lane >> 3) * rs + vt;
      if (role == 0) tile_loads<16, WT_W>(rw, voff, base, 8u * rs, lw, lane);
      else if (role == 1) {
        if (DLY_M1) { vmwait<0>(); if constexpr (landed(N_V2, WIN) < N_V1) set_flag(F_V1 + 4 * lw, lane); set_flag(F_V2 + 4 * lw, lane); delay_ticks(DLY_M1); }
        tile_loads<18, WT_M1>(rw, voff, base, 8u * rs, lw, lane);
      } else {
        if (DLY_M2) { vmwait<0>(); if constexpr (landed(N_V2, WIN) < N_V1) set_flag(F_V1 + 4 * lw, lane); set_flag(F_V2 + 4 * lw, lane); delay_ticks(DLY_M2); }
        tile_loads<18, WT_M2>(rw, voff, base, 8u * rs, lw, lane);
      }
    }
    if (role == 1 && lw == 0) xres = __builtin_amdgcn_raw_buffer_load_b16(rx, (u32)(512 + 4 * c16 + g16) * 2u, 0, 0);
    vmwait<0>();
    set_flag(F_S + 4 * lw, lane);
    STAMP(3);
    wait_flag4<1>(F_S);
    {
      const u32 a0 = L_S + t128_tr(lane, 0, lw), a1 = L_S + t128_tr(lane, 1, lw);
#pragma unroll
      for (int c = 0; c < 16; ++c) B[c] = cat8(lds_tr(a0 + c * 4096), lds_tr(a1 + c * 4096));
      if (role != 0) {
#pragma unroll
        for (int c = 16; c < 18; ++c) B[c] = cat8(lds_tr(a0 + c * 4096), lds_tr(a1 + c * 4096));
      }
    }
    lgkm0();
    STAMP(4);
    cw = (lw == 0 && role != 0) ? 8 : -1;
  } else {
    cw = wv - 4;
    v4u xa; v2u xb;
    if (cw == 0) {
      xa = __builtin_amdgcn_raw_buffer_load_b128(rx, lane * 24, 0, 0);
      xb = __builtin_amdgcn_raw_buffer_load_b64(rx, lane * 24 + 16, 0, 0);
    }
    const int rr = (j - 27) * 8 + x;
    if (role == 1) xres = __builtin_amdgcn_raw_buffer_load_b16(rx, (u32)(64 * cw + 4 * c16 + g16) * 2u, 0, 0);
    u32 keep = 0;
    if (role == 3 && rr < 9 && cw == 0) {
      xres = __builtin_amdgcn_raw_buffer_load_b16(rx, (u32)(64 * rr + 4 * c16 + g16) * 2u, 0, 0);
      keep = __builtin_amdgcn_raw_buffer_load_b32(__builtin_amdgcn_make_buffer_rsrc(a.out, 0, 1152, 0x00020000), 0, 0, 0);
    }
    if (cw == 7) {
      if (lane < 32) lds_st<u32>(L_FL + lane * 4, 0u);
      if (lane < 32) lds_st<u32>(L_ST + lane * 4, 0u);
      lds_st<v4u>(L_ONE + lane * 16, (v4u){0x3f803f80u, 0x3f803f80u, 0x3f803f80u, 0x3f803f80u});
      if (lane < 8) lds_st<v4u>(L_ONE + 1024 + lane * 16, (v4u){0x3f803f80u, 0x3f803f80u, 0x3f803f80u, 0x3f803f80u});
    }
    wg_barrier();
    tag32 = load_epoch(a.ws) + 1u;
    const u32 t10 = tag32 & 0x3ffu, t16 = tag32 & 0xffffu;
    if (cw == 0) {
      float v[12];
#pragma unroll
      for (int i = 0; i < 4; ++i) { v[2 * i] = bf_lo(xa[i]); v[2 * i + 1] = bf_hi(xa[i]); }
#pragma unroll
      for (int i = 0; i < 2; ++i) { v[8 + 2 * i] = bf_lo(xb[i]); v[9 + 2 * i] = bf_hi(xb[i]); }
      float sm = 0.f;
#pragma unroll
      for (int i = 0; i < 12; ++i) sm += v[i];
      sm = x32_sum(x16_sum(r16_sum(sm)));
      const float mean = sm * (1.0f / 576.0f);
      float s1 = 0.f;
#pragma unroll
      for (int i = 0; i < 12; ++i) { const float d = v[i] - mean; s1 += d * d; }
      if (lane >= 48) s1 = 0.f;
      s1 = x32_sum(x16_sum(r16_sum(s1)));
      const float m = rbf(mean);
      const float den = rbf(__builtin_amdgcn_sqrtf(rbf(rbf(s1 * (1.0f / 576.0f)) + 1e-5f)));
      const float rden = __builtin_amdgcn_rcpf(den);
      u32 n[6];
#pragma unroll
      for (int i = 0; i < 6; ++i) {
        const u32 d = pk_bf(v[2 * i] - m, v[2 * i + 1] - m);
        n[i] = pk_bf(bf_lo(d) * rden, bf_hi(d) * rden);
      }
      if (lane < 48) {
#pragma unroll
        for (int i = 0; i < 3; ++i) lds_st<v2u>(L_VEC + lane * 24 + i * 8, (v2u){n[2 * i], n[2 * i + 1]});
      }
      STAMP(1);
      wait_flag4<1>(F_WQ);
      STAMP(2);
      const u32 wb = L_WQ + (u32)(8 * g16 + (c16 >> 2)) * 32u + (u32)(lane & 3) * 8u;
      v4f acc = {0.f, 0.f, 0.f, 0.f};
#pragma unroll
      for (int c = 0; c < 18; ++c)
        acc = mfma32(lds_frag(L_VEC + c * 64 + g16 * 16), cat8(lds_tr(wb + c * 1024), lds_tr(wb + c * 1024 + 128)), acc);
      if (lane < 16) gst1(rws, WS_QB + g * 1024u + (u32)(s * 16 + c16) * 4u, (pk_bf(acc[0], acc[0]) & 0xffff0000u) | t16);
      STAMP(3);
    }
    if (cw == 1) {
      wait_flag4<1>(F_WQ);
      v4u w;
#pragma unroll 1
      for (u32 it = 0; it < SPIN_MAX; ++it) {
        cbar();
        w = gld(rws, WS_QB + g * 1024u + lane * 16u);
        if (none(((w.x ^ t16) | (w.y ^ t16) | (w.z ^ t16) | (w.w ^ t16)) & 0xffffu)) break;
      }
      lds_st<v2u>(L_Q + (lane >> 5) * 272 + (lane & 31) * 8, (v2u){__builtin_amdgcn_perm(w.y, w.x, 0x07060302u), __builtin_amdgcn_perm(w.w, w.z, 0x07060302u)});
      lgkm0();
      set_flag(F_Q, lane);
      STAMP(3);
    }
    wait_flag4<1>(F_K);
    v8b KA[4];
#pragma unroll
    for (int c = 0; c < 4; ++c) KA[c] = lds_frag(L_K + off_b(16 * cw + c16, 4 * c + g16));
    wait_cnt<1>(F_Q, 1);
    STAMP(4);
    {
      v4f acc = {0.f, 0.f, 0.f, 0.f};
#pragma unroll
      for (int c = 0; c < 4; ++c) acc = mfma32(KA[c], lds_frag(L_Q + (lane & 1) * 272 + (32 * c + 8 * g16) * 2), acc);
      v4f sc;
#pragma unroll
      for (int i = 0; i < 4; ++i) sc[i] = rbf(rbf(acc[i]) * 0.08838834764831845f);
      if (c16 < 2) lds_st<v4f>(L_SC + c16 * 512 + (16 * cw + 4 * g16) * 4, sc);
      lgkm0();
      if (lane == 0) __hip_atomic_fetch_add((LAS u32*)(size_t)F_SC, 1u, __ATOMIC_RELAXED, __HIP_MEMORY_SCOPE_WORKGROUP);
      wait_cnt<0>(F_SC, 8);
    }
    STAMP(5);
    {
      const v4f sc = lds_ld<v4f>(L_SC + (lane >> 5) * 512 + (lane & 31) * 16);
      float m = fmaxf(fmaxf(sc[0], sc[1]), fmaxf(sc[2], sc[3]));
      m = x16_max(r16_max(m));
      v4f e;
#pragma unroll
      for (int i = 0; i < 4; ++i) e[i] = __builtin_amdgcn_exp2f((sc[i] - m) * 1.4426950408889634f);
      float l = (e[0] + e[1]) + (e[2] + e[3]);
      l = x16_sum(r16_sum(l));
      lds_st<v2u>(L_P + cw * 576 + (lane >> 5) * 272 + (lane & 31) * 8, (v2u){pk_bf(e[0], e[1]), pk_bf(e[2], e[3])});
      if (cw == 0) {
        const float m1 = __uint_as_float(__builtin_amdgcn_readlane(__float_as_uint(m), 32)), l1 = __uint_as_float(__builtin_amdgcn_readlane(__float_as_uint(l), 32));
        if (lane == 0) gst(rws, WS_OP + (g * 16 + s) * OPS + 1024, pack4((v4f){m, l, m1, l1}, t10));
      }
    }
    v8b PB[4];
#pragma unroll
    for (int c = 0; c < 4; ++c) PB[c] = lds_frag(L_P + cw * 576 + (lane & 1) * 272 + (32 * c + 8 * g16) * 2);
    {
      const int q = c16 >> 2, p = lane & 3;
      v4f acc = {0.f, 0.f, 0.f, 0.f};
      wait_flag4<1>(F_V1);
#pragma unroll
      for (int c = 0; c < 2; ++c) {
        const u32 r0 = 32 * c + 8 * g16 + q;
        acc = mfma32(cat8(lds_tr(L_V + off_b(r0, 2 * cw + (p >> 1)) + 8 * (p & 1)), lds_tr(L_V + off_b(r0 + 4, 2 * cw + (p >> 1)) + 8 * (p & 1))), PB[c], acc);
      }
      wait_flag4<0>(F_V2);
      STAMP(6);
#pragma unroll
      for (int c = 2; c < 4; ++c) {
        const u32 r0 = 32 * c + 8 * g16 + q;
        acc = mfma32(cat8(lds_tr(L_V + off_b(r0, 2 * cw + (p >> 1)) + 8 * (p & 1)), lds_tr(L_V + off_b(r0 + 4, 2 * cw + (p >> 1)) + 8 * (p & 1))), PB[c], acc);
      }
      if (c16 < 2) gst(rws, WS_OP + (g * 16 + s) * OPS + c16 * 512 + (16 * cw + 4 * g16) * 4, pack4(acc, t10));
    }
    STAMP(7);
    asm volatile("" ::"v"(keep));
    if (role == 3) {
      if (rr >= 9 || cw != 0) { STAMP_FLUSH(); return; }
      v4f hs = {0.f, 0.f, 0.f, 0.f}, fs = {0.f, 0.f, 0.f, 0.f};
      const u32 off = (u32)g16 * 2304u + (u32)(64 * rr + 4 * c16) * 4u;
      poll4<2>(rws, WS_HP + off, 4 * 2304u, t10, [&](auto, v4f y) { hs += y; });
      const float h = rbf(bf_lo(xres) + rbf(rs4(hs)));
      STAMP(8);
      poll4<2>(rws, WS_FP + off, 4 * 2304u, t10, [&](auto, v4f y) { fs += y; });
      STAMP(9);
      const u32 o = pk_bf(h + rbf(rs4(fs)), 0.f);
      *(unsigned short*)((char*)a.out + (64 * rr + 4 * c16 + g16) * 2) = (unsigned short)o;
      if (rr == 0 && lane == 0) gst1(rws, WS_EP, tag32);
      STAMP(10);
      STAMP_FLUSH();
      if (rr == 0) AMP_SPIN();
      return;
    }
  }

  const u32 t10 = tag32 & 0x3ffu, t16 = tag32 & 0xffffu;
  if (cw >= 0) {
    const u32 va = L_VEC + (u32)(64 * cw + 4 * c16 + g16) * 2u;
    if (role == 0) {
      const int gg = 2 * x + (cw >> 2), hh = (cw >> 1) & 1;
      const u32 ob = WS_OP + (u32)(gg * 16 + 4 * g16) * OPS;
      v2u st;
#pragma unroll 1
      for (u32 it = 0; it < SPIN_MAX; ++it) {
        cbar();
        st = __builtin_amdgcn_raw_buffer_load_b64(rws, ob + (u32)(c16 & 3) * OPS + 1024u + hh * 8u, 0, 16);
        st ^= t10;
        if (none((st.x | st.y) & 0x3ffu)) break;
      }
      const float m = __uint_as_float(st.x), l = __uint_as_float(st.y);
      float M = fmaxf(m, dppf<0xB1>(m));
      M = fmaxf(M, dppf<0x4E>(M));
      M = x32_max(x16_max(M));
      const float e = __builtin_amdgcn_exp2f((m - M) * 1.4426950408889634f);
      float L = e * l;
      L += dppf<0xB1>(L); L += dppf<0x4E>(L);
      L = x32_sum(x16_sum(L));
      const float f = e * __builtin_amdgcn_rcpf(L);
      const float fq[4] = {dppf<0x00>(f), dppf<0x55>(f), dppf<0xAA>(f), dppf<0xFF>(f)};
      STAMP(8);
      v4f acc = {0.f, 0.f, 0.f, 0.f};
      poll4<4>(rws, ob + hh * 512u + (cw & 1) * 256u + c16 * 16u, OPS, t10, [&](auto i_, v4f y) { acc += fq[decltype(i_)::value] * y; });
      STAMP(9);
      const float o = rs4(acc);
      lds_st<unsigned short>(va, (unsigned short)pk_bf(o, o));
    } else if (role == 1) {
      v4f hs = {0.f, 0.f, 0.f, 0.f};
      poll4<2>(rws, WS_HP + (u32)g16 * 2304u + (u32)(64 * cw + 4 * c16) * 4u, 4 * 2304u, t10, [&](auto, v4f y) { hs += y; });
      STAMP(9);
      const float pre = bf_lo(xres) + rbf(rs4(hs));
      const u32 hb = pk_bf(pre, pre);
      lds_st<unsigned short>(va, (unsigned short)hb);
      const float h = bf_lo(hb);
      const float sr = r16_sum(x16_sum(sw32_add(h, h * h)));
      if ((lane & 31) == 0) lds_st<float>(L_ST + (lane >> 5) * 64 + cw * 4, sr);
    } else {
      u32 w;
#pragma unroll 1
      for (u32 it = 0; it < SPIN_MAX; ++it) {
        cbar();
        w = gld1(rws, WS_GP + (u32)(576 * x + 64 * cw + lane) * 4u);
        if (none((w ^ t16) & 0xffffu)) break;
      }
      STAMP(9);
      lds_st<unsigned short>(L_VEC + (u32)(64 * cw + lane) * 2u, (unsigned short)(w >> 16));
    }
  }
  wg_barrier();
  STAMP(10);
  if (wv >= 4) { STAMP_FLUSH(); return; }

  {
    const u32 ab = ((role == 1 && (c16 & 1)) ? L_ONE : L_VEC) + (u32)g16 * 16u;
    float sv = 0.f;
    if (role == 1) sv = lds_ld<float>(L_ST + (lane & 31) * 4);
    v4f acc = {0.f, 0.f, 0.f, 0.f};
#pragma unroll
    for (int c = 0; c < 16; ++c) acc = mfma32(lds_frag(ab + c * 64), B[c], acc);
    if (role != 0) {
#pragma unroll
      for (int c = 16; c < 18; ++c) acc = mfma32(lds_frag(ab + c * 64), B[c], acc);
    }
    const u32 col = (u32)(64 * jj + 16 * wv + c16);
    if (role == 1) {
      sv = r16_sum(sv);
      const float s1 = __uint_as_float(__builtin_amdgcn_readlane(__float_as_uint(sv), 0)), s2 = __uint_as_float(__builtin_amdgcn_readlane(__float_as_uint(sv), 16));
      const float mean = s1 * (1.0f / 576.0f);
      const float var = fmaxf(s2 * (1.0f / 576.0f) - mean * mean, 0.f);
      const float den = rbf(__builtin_amdgcn_sqrtf(rbf(rbf(var) + 1e-5f)));
      const float pre = rbf((acc[0] - rbf(mean) * acc[1]) * __builtin_amdgcn_rcpf(den));
      const float gv = pre * __builtin_amdgcn_rcpf(1.0f + __builtin_amdgcn_exp2f(pre * -1.4426950408889634f));
      if (lane < 16) gst1(rws, WS_GP + (576u * x + col) * 4u, (pk_bf(gv, gv) & 0xffff0000u) | t16);
    } else {
      if (lane < 16) gst1(rws, (role == 0 ? WS_HP : WS_FP) + x * 2304u + col * 4u, pack1(acc[0], t10));
    }
    STAMP(11);
    STAMP_FLUSH();
  }
}

}

extern "C" void kernel_launch(void* const* d_in, const int* in_sizes, int n_in, void* d_out, int out_size, void* d_ws, size_t ws_size,
                              hipStream_t stream) {
  (void)in_sizes; (void)n_in; (void)out_size; (void)ws_size;
  Args a;
  a.x = d_in[0]; a.kc = d_in[1]; a.vc = d_in[2]; a.wq = d_in[3]; a.wo = d_in[4]; a.w1 = d_in[6]; a.w2 = d_in[7];
  a.out = d_out; a.ws = d_ws;
  (void)hipFuncSetAttribute((const void*)fused_decode_kernel, hipFuncAttributeMaxDynamicSharedMemorySize, (int)L_END);
  hipLaunchKernelGGL(fused_decode_kernel, dim3(256), dim3(768), L_END, stream, a);
}
```

```cpp
#include <hip/hip_runtime.h>
#include <stdint.h>
#include <type_traits>

namespace {

#define LAS __attribute__((address_space(3)))
typedef unsigned int u32;
typedef u32 v4u __attribute__((ext_vector_type(4)));
typedef u32 v2u __attribute__((ext_vector_type(2)));
typedef float v4f __attribute__((ext_vector_type(4)));
typedef float v2f __attribute__((ext_vector_type(2)));
typedef short v4s __attribute__((ext_vector_type(4)));
typedef short v8s __attribute__((ext_vector_type(8)));
typedef __bf16 v8b __attribute__((ext_vector_type(8)));
typedef __bf16 v2b __attribute__((ext_vector_type(2)));
typedef __amdgpu_buffer_rsrc_t rsrc_t;

#define NBURST 6
#define WIN 3
#define WIN_W 6
#define WT_W 6
#define WT_M1 10
#define WT_M2 10
#define RAMP_M1 3
#define RAMP_M2 7
#define GATE_O 1
#define GATE_POLL 1
#define PREFETCH 1
#define CODE_KB 12

constexpr u32 L_WQ = 0, L_K = 18432, L_V = 51200, L_S = 83968, L_FL = 157696;
constexpr u32 L_VEC = L_FL + 192, L_ONE = L_VEC + 1280, L_ST = L_ONE + 1152, L_END = 163840;
constexpr u32 L_Q = 0, L_SC = 1024, L_P = 2048;
constexpr u32 F_WQ = L_FL, F_K = L_FL + 16, F_V1 = L_FL + 32, F_V2 = L_FL + 48, F_S1 = L_FL + 64, F_S2 = L_FL + 80;
constexpr u32 F_Q = L_FL + 96, F_SC = L_FL + 100, F_X = L_FL + 104, F_O = L_FL + 108, F_T = L_FL + 112;
static_assert(L_ST + 128 <= L_END && L_P + 8 * 576 <= L_K, "lds");
constexpr u32 WS_EP = 0, WS_QB = 4096, WS_OP = WS_QB + 16 * 4096, OPS = 1152, WS_HP = WS_OP + 256 * OPS;
constexpr u32 WS_GP = WS_HP + 8 * 2304, WS_FP = WS_GP + 4608 * 4, WS_END = WS_FP + 8 * 2304;
constexpr u32 SPIN_MAX = 1u << 16;

struct Args {
  const void* x; const void* kc; const void* vc; const void* wq; const void* wo; const void* w1; const void* w2;
  void* out; void* ws;
};

template <int N, class F> __device__ __forceinline__ void static_for(F&& f) {
  if constexpr (N > 0) { static_for<N - 1>(f); f(std::integral_constant<int, N - 1>{}); }
}

__device__ __forceinline__ float bf_lo(u32 u) { return __uint_as_float(u << 16); }
__device__ __forceinline__ float bf_hi(u32 u) { return __uint_as_float(u & 0xffff0000u); }
__device__ __forceinline__ u32 pk_bf(float lo, float hi) {
  v2b r = __builtin_convertvector((v2f){lo, hi}, v2b);
  return __builtin_bit_cast(u32, r);
}
__device__ __forceinline__ float rbf(float f) { return bf_lo(pk_bf(f, f)); }

template <int C> __device__ __forceinline__ float dppf(float v) {
  return __builtin_bit_cast(float, __builtin_amdgcn_update_dpp(0, __builtin_bit_cast(int, v), C, 0xf, 0xf, true));
}
__device__ __forceinline__ float r16_sum(float v) { v += dppf<0xB1>(v); v += dppf<0x4E>(v); v += dppf<0x141>(v); v += dppf<0x140>(v); return v; }
__device__ __forceinline__ float r16_max(float v) { v = fmaxf(v, dppf<0xB1>(v)); v = fmaxf(v, dppf<0x4E>(v)); v = fmaxf(v, dppf<0x141>(v)); v = fmaxf(v, dppf<0x140>(v)); return v; }
__device__ __forceinline__ float sw16_add(float a, float b) { auto r = __builtin_amdgcn_permlane16_swap(__float_as_uint(a), __float_as_uint(b), false, false); return __uint_as_float(r[0]) + __uint_as_float(r[1]); }
__device__ __forceinline__ float sw32_add(float a, float b) { auto r = __builtin_amdgcn_permlane32_swap(__float_as_uint(a), __float_as_uint(b), false, false); return __uint_as_float(r[0]) + __uint_as_float(r[1]); }
__device__ __forceinline__ float x16_sum(float v) { return sw16_add(v, v); }
__device__ __forceinline__ float x32_sum(float v) { return sw32_add(v, v); }
__device__ __forceinline__ float x16_max(float v) { auto r = __builtin_amdgcn_permlane16_swap(__float_as_uint(v), __float_as_uint(v), false, false); return fmaxf(__uint_as_float(r[0]), __uint_as_float(r[1])); }
__device__ __forceinline__ float x32_max(float v) { auto r = __builtin_amdgcn_permlane32_swap(__float_as_uint(v), __float_as_uint(v), false, false); return fmaxf(__uint_as_float(r[0]), __uint_as_float(r[1])); }
__device__ __forceinline__ float rs4(v4f a) { return sw32_add(sw16_add(a[0], a[1]), sw16_add(a[2], a[3])); }

template <class T> __device__ __forceinline__ T lds_ld(u32 a) { return *(const LAS T*)(size_t)a; }
template <class T> __device__ __forceinline__ void lds_st(u32 a, T v) { *(LAS T*)(size_t)a = v; }
__device__ __forceinline__ v4s lds_tr(u32 a) { return __builtin_amdgcn_ds_read_tr16_b64_v4i16((LAS v4s*)(size_t)a); }
__device__ __forceinline__ v8b cat8(v4s a, v4s b) { return __builtin_bit_cast(v8b, __builtin_shufflevector(a, b, 0, 1, 2, 3, 4, 5, 6, 7)); }
__device__ __forceinline__ v8b lds_frag(u32 a) { return __builtin_bit_cast(v8b, lds_ld<v4u>(a)); }
__device__ __forceinline__ v4f mfma32(v8b a, v8b b, v4f c) { return __builtin_amdgcn_mfma_f32_16x16x32_bf16(a, b, c, 0, 0, 0); }
__device__ __forceinline__ v4f mfma16(v4s a, v4s b, v4f c) { return __builtin_amdgcn_mfma_f32_16x16x16bf16_1k(a, b, c, 0, 0, 0); }

__device__ __forceinline__ void lgkm0() { asm volatile("s_waitcnt lgkmcnt(0)" ::: "memory"); }
template <int N> __device__ __forceinline__ void vmwait() { asm volatile("s_waitcnt vmcnt(%0)" ::"n"(N) : "memory"); }
__device__ __forceinline__ void cbar() { asm volatile("" ::: "memory"); }
__device__ __forceinline__ void wg_barrier() { lgkm0(); __builtin_amdgcn_s_barrier(); cbar(); }

__device__ __forceinline__ void dma(rsrc_t r, u32 voff, u32 soff, u32 lds) {
  asm volatile("s_mov_b32 m0, %0\n\ts_nop 0\n\tbuffer_load_dwordx4 %1, %2, %3 offen nt lds" ::"s"(lds), "v"(voff), "s"(r), "s"(soff) : "memory");
}

__device__ __forceinline__ void set_flag(u32 a, int lane) {
  if (lane == 0) *(volatile LAS u32*)(size_t)a = 1u;
}
template <int SLEEP> __device__ __forceinline__ void wait_flag4(u32 a) {
#pragma unroll 1
  for (u32 it = 0; it < (1u << 22); ++it) {
    v4u v;
    asm volatile("ds_read_b128 %0, %1\n\ts_waitcnt lgkmcnt(0)" : "=v"(v) : "v"(a) : "memory");
    if (__builtin_amdgcn_readfirstlane(v.x & v.y & v.z & v.w)) break;
    if (SLEEP) __builtin_amdgcn_s_sleep(SLEEP);
  }
}
template <int SLEEP> __device__ __forceinline__ void wait_cnt(u32 a, u32 n) {
#pragma unroll 1
  for (u32 it = 0; it < (1u << 22); ++it) {
    u32 v;
    asm volatile("ds_read_b32 %0, %1\n\ts_waitcnt lgkmcnt(0)" : "=v"(v) : "v"(a) : "memory");
    if (__builtin_amdgcn_readfirstlane(v) >= n) break;
    if (SLEEP) __builtin_amdgcn_s_sleep(SLEEP);
  }
}
__device__ __forceinline__ void count_up(u32 a, int lane) {
  if (lane == 0) __hip_atomic_fetch_add((LAS u32*)(size_t)a, 1u, __ATOMIC_RELAXED, __HIP_MEMORY_SCOPE_WORKGROUP);
}

__device__ __forceinline__ v4u gld(rsrc_t r, u32 off) { return __builtin_amdgcn_raw_buffer_load_b128(r, off, 0, 16); }
__device__ __forceinline__ u32 gld1(rsrc_t r, u32 off) { return __builtin_amdgcn_raw_buffer_load_b32(r, off, 0, 16); }
__device__ __forceinline__ void gst(rsrc_t r, u32 off, v4u v) { __builtin_amdgcn_raw_buffer_store_b128(v, r, off, 0, 16); }
__device__ __forceinline__ void gst1(rsrc_t r, u32 off, u32 v) { __builtin_amdgcn_raw_buffer_store_b32(v, r, off, 0, 16); }
__device__ __forceinline__ u32 pack1(float v, u32 t) { return ((__float_as_uint(v) + 0x200u) & ~0x3ffu) | t; }
__device__ __forceinline__ v4u pack4(v4f v, u32 t) { return (v4u){pack1(v[0], t), pack1(v[1], t), pack1(v[2], t), pack1(v[3], t)}; }
__device__ __forceinline__ bool none(u32 bad) { return __all(bad == 0u); }
__device__ __forceinline__ u32 low10(v4u y) { return (y.x | y.y | y.z | y.w) & 0x3ffu; }

template <int N> __device__ __forceinline__ void poll_all(rsrc_t r, u32 off, u32 stride, u32 t, v4f (&y)[N]) {
#pragma unroll 1
  for (u32 it = 0; it < SPIN_MAX; ++it) {
    cbar();
    v4u w[N];
    u32 bad = 0;
    static_for<N>([&](auto i_) { constexpr int i = decltype(i_)::value; w[i] = gld(r, off + i * stride); });
    static_for<N>([&](auto i_) { constexpr int i = decltype(i_)::value; w[i] ^= t; bad |= low10(w[i]); y[i] = __builtin_bit_cast(v4f, w[i]); });
    if (none(bad)) break;
  }
}
template <int N> __device__ __forceinline__ void poll_pend(rsrc_t r, u32 off, u32 stride, u32 t, v4f (&y)[N]) {
  u32 pend = (1u << N) - 1u;
  v4u w[N];
#pragma unroll 1
  for (u32 it = 0; it < SPIN_MAX; ++it) {
    cbar();
    static_for<N>([&](auto i_) { constexpr int i = decltype(i_)::value; if (pend & (1u << i)) w[i] = gld(r, off + i * stride); });
    static_for<N>([&](auto i_) {
      constexpr int i = decltype(i_)::value;
      if (pend & (1u << i)) {
        w[i] ^= t;
        if (none(low10(w[i]))) pend &= ~(1u << i);
      }
    });
    if (pend == 0u) break;
  }
  static_for<N>([&](auto i_) { constexpr int i = decltype(i_)::value; y[i] = __builtin_bit_cast(v4f, w[i]); });
}

#define STAMP(i) do { } while (0)
#define STAMP_DECL do { } while (0)
#define STAMP_FLUSH() do { } while (0)
#define AMP_SPIN() do { } while (0)

__device__ __forceinline__ u32 t128_tr(int lane, int t, int nb) {
  const int g = lane >> 4, q = (lane & 15) >> 2, p = lane & 3;
  const int r = 8 * g + 4 * t + q;
  const int f = (q >> 1) | (((t ^ g) & 1) << 1);
  return (u32)(r * 128 + ((nb ^ f) << 5) + ((p >> 1) << 4) + ((p & 1) << 3));
}
__device__ __forceinline__ u32 t128_tr16(int lane, int nb) {
  const int g = lane >> 4, q = (lane & 15) >> 2, p = lane & 3;
  const int r = 4 * g + q;
  const int f = (q >> 1) | (((g ^ (g >> 1)) & 1) << 1);
  return (u32)(r * 128 + ((nb ^ f) << 5) + ((p >> 1) << 4) + ((p & 1) << 3));
}
__device__ __forceinline__ u32 off_b(u32 row, u32 ch) { return 256u * row + 16u * (ch ^ (((row & 3) << 2) | ((row >> 2) & 3))); }

constexpr int N_WQ = 5, N_K = 13, N_V1 = 17, N_V2 = 21;
template <int WIN_, int NT_, int WT_, int RAMP_> struct Sched {
  static constexpr int NT = NT_, RAMP = RAMP_, WT = WT_;
  static constexpr int win(int n) { return n <= N_V2 + RAMP_ ? WIN_ : (WIN_ + (n - N_V2 - RAMP_) < WT_ ? WIN_ + (n - N_V2 - RAMP_) : WT_); }
  static constexpr int landed(int n) { return n < NBURST ? 0 : (n - win(n) > 0 ? n - win(n) : 0); }
};
template <class C, int n> __device__ __forceinline__ void after(int lane, int lw) {
  if constexpr (n >= NBURST && n > C::win(n) && C::landed(n) > C::landed(n - 1)) vmwait<C::win(n)>();
  constexpr int a = C::landed(n - 1), b = C::landed(n);
  if constexpr (a < N_WQ && b >= N_WQ) set_flag(F_WQ + 4 * lw, lane);
  if constexpr (a < N_K && b >= N_K) set_flag(F_K + 4 * lw, lane);
  if constexpr (a < N_V1 && b >= N_V1) set_flag(F_V1 + 4 * lw, lane);
  if constexpr (a < N_V2 && b >= N_V2) set_flag(F_V2 + 4 * lw, lane);
}

__device__ __forceinline__ u32 load_epoch(const void* ws) {
  return *(const volatile u32*)ws;
}

__global__ void __launch_bounds__(768, 3) fused_decode_kernel(Args a) {
  extern __shared__ __attribute__((aligned(16))) unsigned char smem[];
  const int tid = threadIdx.x, lane = tid & 63;
  const int wv = __builtin_amdgcn_readfirstlane(tid >> 6);
  const int b = blockIdx.x, x = b & 7, j = b >> 3;
  const int g = 2 * x + (j >> 4), s = j & 15;
  const int role = j < 9 ? 0 : (j < 18 ? 1 : (j < 27 ? 2 : 3));
  const int jj = j - 9 * role;
  const int g16 = lane >> 4, c16 = lane & 15;
  STAMP_DECL;
  STAMP(0);
  int cw;
  u32 tag32;
  u32 xres = 0;
  v8b B[18];
  const rsrc_t rws = __builtin_amdgcn_make_buffer_rsrc(a.ws, 0, WS_END, 0x00020000);
  const rsrc_t rx = __builtin_amdgcn_make_buffer_rsrc((void*)a.x, 0, 1152, 0x00020000);

  if (wv < 4) {
    const int lw = wv;
    const u32 vt = (u32)(((lane & 7) ^ (((lane >> 4) & 1) << 1) ^ (((lane >> 5) & 1) << 2)) * 16);
    if (role == 1 && lw == 0) xres = __builtin_amdgcn_raw_buffer_load_b16(rx, (u32)(512 + 4 * c16 + g16) * 2u, 0, 0);
    {
      const rsrc_t rq = __builtin_amdgcn_make_buffer_rsrc((void*)a.wq, 0, 576 * 4096 * 2, 0x00020000);
      const u32 voff = (u32)(lane >> 3) * 8192u + vt;
      const u32 base = (u32)(s & 3) * 144u * 8192u + (u32)(g * 256 + (s >> 2) * 64) * 2u;
      static_for<5>([&](auto k_) {
        constexpr int k = decltype(k_)::value;
        const u32 i = (u32)min(lw + 4 * k, 17);
        dma(rq, voff ^ ((i & 1u) << 6), base + i * 65536u, L_WQ + i * 1024u);
      });
    }
    static_assert(NBURST > 5, "no flag may be published before the first barrier");
    __builtin_amdgcn_s_barrier();
    cbar();
    const u32 vkv = ((u32)(lane >> 4) * 256u + (u32)(((lane & 15) ^ ((lane >> 4) << 2)) * 16)) ^ (u32)(lw << 4);
    const u32 bkv = (u32)(g * 8192 + 6144 + s * 128) * 256u + lw * 1024u;
    const rsrc_t rk = __builtin_amdgcn_make_buffer_rsrc((void*)a.kc, 0, 16 * 8192 * 256, 0x00020000);
    const rsrc_t rv = __builtin_amdgcn_make_buffer_rsrc((void*)a.vc, 0, 16 * 8192 * 256, 0x00020000);
    const void* wp = role == 0 ? a.wo : (role == 1 ? a.w1 : a.w2);
    const u32 wbytes = role == 0 ? 4096u * 576u * 2u : 4608u * 576u * 2u;
    const rsrc_t rw = __builtin_amdgcn_make_buffer_rsrc((void*)wp, 0, wbytes, 0x00020000);
    const u32 rs = role == 1 ? 9216u : 1152u;
    const u32 wbase = role == 0 ? (u32)x * 512u * 1152u + (u32)jj * 128u
                    : role == 1 ? (u32)(x * 9 + jj) * 128u
                                : (u32)x * 576u * 1152u + (u32)jj * 128u;
    const u32 wvoff = (u32)(lane >> 3) * rs + vt;
    auto stream = [&](auto c_) {
      using C = decltype(c_);
      static_for<8>([&](auto k_) {
        constexpr int k = decltype(k_)::value;
        dma(rk, vkv, bkv + k * 4096u, L_K + lw * 1024u + k * 4096u);
        after<C, N_WQ + k + 1>(lane, lw);
      });
      STAMP(1);
      static_for<8>([&](auto k_) {
        constexpr int k = decltype(k_)::value;
        dma(rv, vkv, bkv + k * 4096u, L_V + lw * 1024u + k * 4096u);
        after<C, N_K + k + 1>(lane, lw);
      });
      STAMP(2);
      if constexpr (C::NT == 0) {
        if constexpr (C::landed(N_V2) < N_V1) { vmwait<4>(); set_flag(F_V1 + 4 * lw, lane); }
        vmwait<0>();
        set_flag(F_V2 + 4 * lw, lane);
      } else {
        static_assert(C::landed(N_V2 + C::NT) >= N_V2, "V flags must be published inside the tile phase");
        static_for<C::NT>([&](auto k_) {
          constexpr int k = decltype(k_)::value;
          const u32 i = lw + 4 * k;
          if constexpr (GATE_O && C::RAMP == k && C::NT == 18 && C::WT == WT_M1 && C::RAMP == RAMP_M1) { if (role == 1) wait_cnt<1>(F_O, 8); }
          dma(rw, wvoff ^ ((i & 1u) << 6), wbase + i * 8u * rs, L_S + i * 1024u);
          after<C, N_V2 + k + 1>(lane, lw);
        });
        const u32 a0 = L_S + t128_tr(lane, 0, lw), a1 = L_S + t128_tr(lane, 1, lw);
        constexpr int NH = C::NT / 2;
        vmwait<(C::NT - NH < C::win(N_V2 + C::NT) ? C::NT - NH : C::win(N_V2 + C::NT))>();
        set_flag(F_S1 + 4 * lw, lane);
        wait_flag4<1>(F_S1);
#pragma unroll
        for (int c = 0; c < NH; ++c) B[c] = cat8(lds_tr(a0 + c * 4096), lds_tr(a1 + c * 4096));
        vmwait<0>();
        set_flag(F_S2 + 4 * lw, lane);
        STAMP(3);
        wait_flag4<1>(F_S2);
#pragma unroll
        for (int c = NH; c < C::NT; ++c) B[c] = cat8(lds_tr(a0 + c * 4096), lds_tr(a1 + c * 4096));
      }
    };
    if (role == 0) stream(Sched<WIN_W, 16, WT_W, 0>{});
    else if (role == 1) stream(Sched<WIN, 18, WT_M1, RAMP_M1>{});
    else if (role == 2) stream(Sched<WIN, 18, WT_M2, RAMP_M2>{});
    else { stream(Sched<WIN, 0, WIN, 0>{}); STAMP_FLUSH(); return; }
    wait_cnt<1>(F_T, 1);
    tag32 = lds_ld<u32>(F_T + 4);
    lgkm0();
    STAMP(4);
    cw = (lw == 0 && role != 0) ? 8 : -1;
  } else {
    cw = wv - 4;
    v4u xa; v2u xb, xq;
    if (cw == 0) {
      xa = __builtin_amdgcn_raw_buffer_load_b128(rx, lane * 24, 0, 0);
      xb = __builtin_amdgcn_raw_buffer_load_b64(rx, lane * 24 + 16, 0, 0);
      xq = __builtin_amdgcn_raw_buffer_load_b64(rx, (s & 3) * 288 + lane * 8, 0, 0);
    }
    const u32 ep = __builtin_amdgcn_raw_buffer_load_b32(rws, WS_EP, 0, 0);
    const int rr = (j - 27) * 8 + x;
    if (role == 1) xres = __builtin_amdgcn_raw_buffer_load_b16(rx, (u32)(64 * cw + 4 * c16 + g16) * 2u, 0, 0);
    u32 keep = 0;
    if (role == 3 && rr < 9 && cw == 0) {
      xres = __builtin_amdgcn_raw_buffer_load_b16(rx, (u32)(64 * rr + 4 * c16 + g16) * 2u, 0, 0);
      keep = __builtin_amdgcn_raw_buffer_load_b32(__builtin_amdgcn_make_buffer_rsrc(a.out, 0, 1152, 0x00020000), 0, 0, 0);
    }
    if (cw == 7) {
      if (lane < 48) lds_st<u32>(L_FL + lane * 4, 0u);
      if (lane < 32) lds_st<u32>(L_ST + lane * 4, 0u);
      lds_st<v4u>(L_ONE + lane * 16, (v4u){0x3f803f80u, 0x3f803f80u, 0x3f803f80u, 0x3f803f80u});
      if (lane < 8) lds_st<v4u>(L_ONE + 1024 + lane * 16, (v4u){0x3f803f80u, 0x3f803f80u, 0x3f803f80u, 0x3f803f80u});
    }
    wg_barrier();
    if (cw == 0) {
      float v[12];
#pragma unroll
      for (int i = 0; i < 4; ++i) { v[2 * i] = bf_lo(xa[i]); v[2 * i + 1] = bf_hi(xa[i]); }
#pragma unroll
      for (int i = 0; i < 2; ++i) { v[8 + 2 * i] = bf_lo(xb[i]); v[9 + 2 * i] = bf_hi(xb[i]); }
      const float x0 = __uint_as_float(__builtin_amdgcn_readfirstlane(__float_as_uint(v[0])));
      float s1 = 0.f, s2 = 0.f;
#pragma unroll
      for (int i = 0; i < 12; ++i) { const float d = v[i] - x0; s1 += d; s2 += d * d; }
      if (lane >= 48) { s1 = 0.f; s2 = 0.f; }
      const float sr = r16_sum(x16_sum(sw32_add(s1, s2)));
      auto r2 = __builtin_amdgcn_permlane32_swap(__float_as_uint(sr), __float_as_uint(sr), false, false);
      const float md = __uint_as_float(r2[0]) * (1.0f / 576.0f);
      const float var = fmaxf(__uint_as_float(r2[1]) * (1.0f / 576.0f) - md * md, 0.f);
      const float m = rbf(x0 + md);
      const float den = rbf(__builtin_amdgcn_sqrtf(rbf(rbf(var) + 1e-5f)));
      const float rden = __builtin_amdgcn_rcpf(den);
      v2u n;
#pragma unroll
      for (int i = 0; i < 2; ++i) {
        const u32 d = pk_bf(bf_lo(xq[i]) - m, bf_hi(xq[i]) - m);
        n[i] = pk_bf(bf_lo(d) * rden, bf_hi(d) * rden);
      }
      if (lane < 36) lds_st<v2u>(L_VEC + lane * 8, n);
      lgkm0();
      set_flag(F_X, lane);
      STAMP(1);
    }
    tag32 = __builtin_amdgcn_readfirstlane(ep) + 1u;
    const u32 t10 = tag32 & 0x3ffu;
    if (cw < 4) {
      const u32 b0 = L_WQ + t128_tr(lane, 0, cw), b1 = L_WQ + t128_tr(lane, 1, cw), b2 = L_WQ + 16384 + t128_tr16(lane, cw);
      wait_cnt<1>(F_X, 1);
      v8b A[4];
#pragma unroll
      for (int c = 0; c < 4; ++c) A[c] = lds_frag(L_VEC + c * 64 + g16 * 16);
      const v4s A4 = lds_ld<v4s>(L_VEC + 256 + g16 * 8);
      wait_flag4<1>(F_WQ);
      STAMP(2);
      v4f acc = {0.f, 0.f, 0.f, 0.f};
#pragma unroll
      for (int c = 0; c < 4; ++c) acc = mfma32(A[c], cat8(lds_tr(b0 + c * 4096), lds_tr(b1 + c * 4096)), acc);
      acc = mfma16(A4, lds_tr(b2), acc);
      if (lane < 16) gst1(rws, WS_QB + g * 4096u + (u32)(s & 3) * 1024u + (u32)((s >> 2) * 64 + cw * 16 + c16) * 4u, pack1(acc[0], t10));
      STAMP(3);
    }
    if (cw == 7) { if (lane == 0) lds_st<v2u>(F_T, (v2u){1u, tag32}); }
    if (cw == 4) {
      wait_flag4<1>(F_WQ);
      v4f y[4];
      poll_all<4>(rws, WS_QB + g * 4096u + lane * 16u, 1024u, t10, y);
      const v4f q = (y[0] + y[1]) + (y[2] + y[3]);
      lds_st<v2u>(L_Q + (lane >> 5) * 272 + (lane & 31) * 8, (v2u){pk_bf(q[0], q[1]), pk_bf(q[2], q[3])});
      lgkm0();
      set_flag(F_Q, lane);
      STAMP(3);
    }
    wait_flag4<1>(F_K);
    v8b KA[4];
#pragma unroll
    for (int c = 0; c < 4; ++c) KA[c] = lds_frag(L_K + off_b(16 * cw + c16, 4 * c + g16));
    wait_cnt<1>(F_Q, 1);
    STAMP(4);
    {
      v4f acc = {0.f, 0.f, 0.f, 0.f};
#pragma unroll
      for (int c = 0; c < 4; ++c) acc = mfma32(KA[c], lds_frag(L_Q + (lane & 1) * 272 + (32 * c + 8 * g16) * 2), acc);
      v4f sc;
#pragma unroll
      for (int i = 0; i < 4; ++i) sc[i] = rbf(rbf(acc[i]) * 0.08838834764831845f);
      if (c16 < 2) lds_st<v4f>(L_SC + c16 * 512 + (16 * cw + 4 * g16) * 4, sc);
      lgkm0();
      count_up(F_SC, lane);
      wait_cnt<0>(F_SC, 8);
    }
    STAMP(5);
    {
      const v4f sc = lds_ld<v4f>(L_SC + (lane >> 5) * 512 + (lane & 31) * 16);
      float m = fmaxf(fmaxf(sc[0], sc[1]), fmaxf(sc[2], sc[3]));
      m = x16_max(r16_max(m));
      v4f e;
#pragma unroll
      for (int i = 0; i < 4; ++i) e[i] = __builtin_amdgcn_exp2f((sc[i] - m) * 1.4426950408889634f);
      float l = (e[0] + e[1]) + (e[2] + e[3]);
      l = x16_sum(r16_sum(l));
      lds_st<v2u>(L_P + cw * 576 + (lane >> 5) * 272 + (lane & 31) * 8, (v2u){pk_bf(e[0], e[1]), pk_bf(e[2], e[3])});
      if (cw == 0) {
        const float m1 = __uint_as_float(__builtin_amdgcn_readlane(__float_as_uint(m), 32)), l1 = __uint_as_float(__builtin_amdgcn_readlane(__float_as_uint(l), 32));
        if (lane == 0) gst(rws, WS_OP + (g * 16 + s) * OPS + 1024, pack4((v4f){m, l, m1, l1}, t10));
      }
    }
    v8b PB[4];
#pragma unroll
    for (int c = 0; c < 4; ++c) PB[c] = lds_frag(L_P + cw * 576 + (lane & 1) * 272 + (32 * c + 8 * g16) * 2);
    {
      const int q = c16 >> 2, p = lane & 3;
      v4f acc = {0.f, 0.f, 0.f, 0.f};
      wait_flag4<1>(F_V1);
#pragma unroll
      for (int c = 0; c < 2; ++c) {
        const u32 r0 = 32 * c + 8 * g16 + q;
        acc = mfma32(cat8(lds_tr(L_V + off_b(r0, 2 * cw + (p >> 1)) + 8 * (p & 1)), lds_tr(L_V + off_b(r0 + 4, 2 * cw + (p >> 1)) + 8 * (p & 1))), PB[c], acc);
      }
      wait_flag4<0>(F_V2);
      STAMP(6);
#pragma unroll
      for (int c = 2; c < 4; ++c) {
        const u32 r0 = 32 * c + 8 * g16 + q;
        acc = mfma32(cat8(lds_tr(L_V + off_b(r0, 2 * cw + (p >> 1)) + 8 * (p & 1)), lds_tr(L_V + off_b(r0 + 4, 2 * cw + (p >> 1)) + 8 * (p & 1))), PB[c], acc);
      }
      if (c16 < 2) gst(rws, WS_OP + (g * 16 + s) * OPS + c16 * 512 + (16 * cw + 4 * g16) * 4, pack4(acc, t10));
      if (GATE_O && role == 1) count_up(F_O, lane);
    }
    STAMP(7);
    asm volatile("" ::"v"(keep));
    if (role == 3) {
      if (rr >= 9 || cw != 0) { STAMP_FLUSH(); return; }
      const u32 off = (u32)g16 * 2304u + (u32)(64 * rr + 4 * c16) * 4u;
      const u32 oa = (u32)(64 * rr + 4 * c16 + g16) * 2u;
      v4f y[2];
      poll_all<2>(rws, WS_HP + off, 4 * 2304u, t10, y);
      const float h = rbf(bf_lo(xres) + rbf(rs4(y[0] + y[1])));
      STAMP(8);
      poll_all<2>(rws, WS_FP + off, 4 * 2304u, t10, y);
      STAMP(9);
      const u32 o = pk_bf(h + rbf(rs4(y[0] + y[1])), 0.f);
      *(unsigned short*)((char*)a.out + oa) = (unsigned short)o;
      if (rr == 0 && lane == 0) gst1(rws, WS_EP, tag32);
      STAMP(10);
      STAMP_FLUSH();
      if (rr == 0) AMP_SPIN();
      return;
    }
  }

  const u32 t10 = tag32 & 0x3ffu, t16 = tag32 & 0xffffu;
  if (cw >= 0) {
    const u32 va = L_VEC + (u32)(64 * cw + 4 * c16 + g16) * 2u;
    if (role == 0) {
      const int gg = 2 * x + (cw >> 2), hh = (cw >> 1) & 1;
      const u32 ob = WS_OP + (u32)(gg * 16 + 4 * g16) * OPS;
      v2u st;
#pragma unroll 1
      for (u32 it = 0; it < SPIN_MAX; ++it) {
        cbar();
        st = __builtin_amdgcn_raw_buffer_load_b64(rws, ob + (u32)(c16 & 3) * OPS + 1024u + hh * 8u, 0, 16);
        st ^= t10;
        if (none((st.x | st.y) & 0x3ffu)) break;
      }
      const float m = __uint_as_float(st.x), l = __uint_as_float(st.y);
      float M = fmaxf(m, dppf<0xB1>(m));
      M = fmaxf(M, dppf<0x4E>(M));
      M = x32_max(x16_max(M));
      const float e = __builtin_amdgcn_exp2f((m - M) * 1.4426950408889634f);
      float L = e * l;
      L += dppf<0xB1>(L); L += dppf<0x4E>(L);
      L = x32_sum(x16_sum(L));
      const float f = e * __builtin_amdgcn_rcpf(L);
      const float fq[4] = {dppf<0x00>(f), dppf<0x55>(f), dppf<0xAA>(f), dppf<0xFF>(f)};
      STAMP(8);
      if (GATE_POLL) wait_flag4<1>(F_S2);
      v4f y[4];
      poll_pend<4>(rws, ob + hh * 512u + (cw & 1) * 256u + c16 * 16u, OPS, t10, y);
      STAMP(9);
      const float o = rs4((fq[0] * y[0] + fq[1] * y[1]) + (fq[2] * y[2] + fq[3] * y[3]));
      lds_st<unsigned short>(va, (unsigned short)pk_bf(o, o));
    } else if (role == 1) {
      if (GATE_POLL && cw < 8) wait_flag4<1>(F_S2);
      v4f y[2];
      poll_all<2>(rws, WS_HP + (u32)g16 * 2304u + (u32)(64 * cw + 4 * c16) * 4u, 4 * 2304u, t10, y);
      STAMP(9);
      const float pre = bf_lo(xres) + rbf(rs4(y[0] + y[1]));
      const u32 hb = pk_bf(pre, pre);
      lds_st<unsigned short>(va, (unsigned short)hb);
      const float h = bf_lo(hb);
      const float sr = r16_sum(x16_sum(sw32_add(h, h * h)));
      if ((lane & 31) == 0) lds_st<float>(L_ST + (lane >> 5) * 64 + cw * 4, sr);
    } else {
      if (GATE_POLL && cw < 8) wait_flag4<1>(F_S2);
      u32 w;
#pragma unroll 1
      for (u32 it = 0; it < SPIN_MAX; ++it) {
        cbar();
        w = gld1(rws, WS_GP + (u32)(576 * x + 64 * cw + lane) * 4u);
        if (none((w ^ t16) & 0xffffu)) break;
      }
      STAMP(9);
      lds_st<unsigned short>(L_VEC + (u32)(64 * cw + lane) * 2u, (unsigned short)(w >> 16));
    }
  }
  if (wv >= 4) { wg_barrier(); STAMP(10); STAMP_FLUSH(); return; }

  {
    const u32 col = (u32)(64 * jj + 16 * wv + c16);
    u32 ab = ((role == 1 && (c16 & 1)) ? L_ONE : L_VEC) + (u32)g16 * 16u;
    u32 oaddr = role == 1 ? WS_GP + (576u * x + col) * 4u : (role == 0 ? WS_HP : WS_FP) + x * 2304u + col * 4u;
    if (lane >= 16) oaddr = 0x7ffffff0u;
    u32 sa = L_ST + (u32)(lane & 31) * 4u;
    asm volatile("" : "+v"(ab), "+v"(oaddr), "+v"(sa));
    v4f acc;
    v4u A0, A1, A2, A3, A4, A5, A6;
#define RD(r, o) "ds_read_b128 %[a" #r "], %[ab] offset:" #o "\n\t"
#define WT(n) "s_waitcnt lgkmcnt(" #n ")\n\t"
#define MF0(r, b) "v_mfma_f32_16x16x32_bf16 %[acc], %[a" #r "], %[b" #b "], 0\n\t"
#define MF(r, b) "v_mfma_f32_16x16x32_bf16 %[acc], %[a" #r "], %[b" #b "], %[acc]\n\t"
#define CH_OUT [acc] "=&v"(acc), [a0] "=&v"(A0), [a1] "=&v"(A1), [a2] "=&v"(A2), [a3] "=&v"(A3), [a4] "=&v"(A4), [a5] "=&v"(A5), [a6] "=&v"(A6)
#define CH_IN16 [ab] "v"(ab), [b0] "v"(B[0]), [b1] "v"(B[1]), [b2] "v"(B[2]), [b3] "v"(B[3]), [b4] "v"(B[4]), [b5] "v"(B[5]), [b6] "v"(B[6]), \
                [b7] "v"(B[7]), [b8] "v"(B[8]), [b9] "v"(B[9]), [b10] "v"(B[10]), [b11] "v"(B[11]), [b12] "v"(B[12]), [b13] "v"(B[13]), \
                [b14] "v"(B[14]), [b15] "v"(B[15])
#define CH_IN18 CH_IN16, [b16] "v"(B[16]), [b17] "v"(B[17])
    if (role == 0) {
      wg_barrier();
      STAMP(10);
      asm volatile(""
                   RD(0, 0) RD(1, 64) RD(2, 128) RD(3, 192) RD(4, 256) RD(5, 320) RD(6, 384) WT(6) MF0(0, 0) RD(0, 448) WT(6)
                   MF(1, 1) RD(1, 512) WT(6) MF(2, 2) RD(2, 576) WT(6) MF(3, 3) RD(3, 640) WT(6) MF(4, 4) RD(4, 704) WT(6)
                   MF(5, 5) RD(5, 768) WT(6) MF(6, 6) RD(6, 832) WT(6) MF(0, 7) RD(0, 896) WT(6) MF(1, 8) RD(1, 960) WT(6)
                   MF(2, 9) WT(5) MF(3, 10) WT(4) MF(4, 11) WT(3) MF(5, 12) WT(2) MF(6, 13) WT(1) MF(0, 14) WT(0) MF(1, 15)
                   "s_nop 7"
                   : CH_OUT : CH_IN16 : "memory");
      gst1(rws, oaddr, pack1(acc[0], t10));
    } else if (role == 2) {
      wg_barrier();
      STAMP(10);
      asm volatile(""
                   RD(0, 0) RD(1, 64) RD(2, 128) RD(3, 192) RD(4, 256) RD(5, 320) RD(6, 384) WT(6) MF0(0, 0) RD(0, 448) WT(6)
                   MF(1, 1) RD(1, 512) WT(6) MF(2, 2) RD(2, 576) WT(6) MF(3, 3) RD(3, 640) WT(6) MF(4, 4) RD(4, 704) WT(6)
                   MF(5, 5) RD(5, 768) WT(6) MF(6, 6) RD(6, 832) WT(6) MF(0, 7) RD(0, 896) WT(6) MF(1, 8) RD(1, 960) WT(6)
                   MF(2, 9) RD(2, 1024) WT(6) MF(3, 10) RD(3, 1088) WT(6) MF(4, 11) WT(5) MF(5, 12) WT(4) MF(6, 13) WT(3)
                   MF(0, 14) WT(2) MF(1, 15) WT(1) MF(2, 16) WT(0) MF(3, 17) "s_nop 7"
                   : CH_OUT : CH_IN18 : "memory");
      gst1(rws, oaddr, pack1(acc[0], t10));
    } else {
      float mean, rden;
      wg_barrier();
      STAMP(10);
      asm volatile("ds_read_b32 %[mean], %[sa]\n\t"
                   RD(0, 0) RD(1, 64) RD(2, 128) RD(3, 192) RD(4, 256) RD(5, 320) RD(6, 384) WT(6) MF0(0, 0) RD(0, 448)
                   "v_add_f32_dpp %[mean], %[mean], %[mean] quad_perm:[1,0,3,2] row_mask:0xf bank_mask:0xf bound_ctrl:1\n\t" WT(6)
                   MF(1, 1) RD(1, 512)
                   "v_add_f32_dpp %[mean], %[mean], %[mean] quad_perm:[2,3,0,1] row_mask:0xf bank_mask:0xf bound_ctrl:1\n\t" WT(6)
                   MF(2, 2) RD(2, 576)
                   "v_add_f32_dpp %[mean], %[mean], %[mean] row_half_mirror row_mask:0xf bank_mask:0xf bound_ctrl:1\n\t" WT(6)
                   MF(3, 3) RD(3, 640)
                   "v_add_f32_dpp %[mean], %[mean], %[mean] row_mirror row_mask:0xf bank_mask:0xf bound_ctrl:1\n\t" WT(6) MF(4, 4)
                   RD(4, 704)
                   "v_mov_b32 %[rden], %[mean]\n\t" WT(6) MF(5, 5) RD(5, 768)
                   "v_permlane16_swap_b32 %[mean], %[rden]\n\t" WT(6) MF(6, 6) RD(6, 832)
                   "v_mul_f32 %[mean], 0x3ae38e39, %[mean]\n\t" WT(6) MF(0, 7) RD(0, 896)
                   "v_mul_f32 %[rden], 0x3ae38e39, %[rden]\n\t" WT(6) MF(1, 8) RD(1, 960)
                   "v_fma_f32 %[rden], -%[mean], %[mean], %[rden]\n\t" WT(6) MF(2, 9) RD(2, 1024)
                   "v_max_f32 %[rden], 0, %[rden]\n\t"
                   "v_cvt_pk_bf16_f32 %[mean], %[mean], %[mean]\n\t" WT(6) MF(3, 10) RD(3, 1088)
                   "v_cvt_pk_bf16_f32 %[rden], %[rden], %[rden]\n\t"
                   "v_lshlrev_b32 %[mean], 16, %[mean]\n\t" WT(6) MF(4, 11)
                   "v_lshlrev_b32 %[rden], 16, %[rden]\n\t" WT(5) MF(5, 12)
                   "v_add_f32 %[rden], 0x3727c5ac, %[rden]\n\t" WT(4) MF(6, 13)
                   "v_cvt_pk_bf16_f32 %[rden], %[rden], %[rden]\n\t"
                   "v_lshlrev_b32 %[rden], 16, %[rden]\n\t" WT(3) MF(0, 14)
                   "v_sqrt_f32 %[rden], %[rden]\n\t" WT(2) MF(1, 15)
                   "v_cvt_pk_bf16_f32 %[rden], %[rden], %[rden]\n\t"
                   "v_lshlrev_b32 %[rden], 16, %[rden]\n\t" WT(1) MF(2, 16)
                   "v_rcp_f32 %[rden], %[rden]\n\t" WT(0) MF(3, 17) "s_nop 7"
                   : CH_OUT, [mean] "=&v"(mean), [rden] "=&v"(rden) : CH_IN18, [sa] "v"(sa) : "memory");
      const float pre = rbf((acc[0] - mean * acc[1]) * rden);
      const float gv = pre * __builtin_amdgcn_rcpf(1.0f + __builtin_amdgcn_exp2f(pre * -1.4426950408889634f));
      gst1(rws, oaddr, (pk_bf(gv, gv) & 0xffff0000u) | t16);
    }
    STAMP(11);
    STAMP_FLUSH();
  }
}

}

extern "C" void kernel_launch(void* const* d_in, const int* in_sizes, int n_in, void* d_out, int out_size, void* d_ws, size_t ws_size,
                              hipStream_t stream) {
  (void)in_sizes; (void)n_in; (void)out_size; (void)ws_size;
  Args a;
  a.x = d_in[0]; a.kc = d_in[1]; a.vc = d_in[2]; a.wq = d_in[3]; a.wo = d_in[4]; a.w1 = d_in[6]; a.w2 = d_in[7];
  a.out = d_out; a.ws = d_ws;
  (void)hipFuncSetAttribute((const void*)fused_decode_kernel, hipFuncAttributeMaxDynamicSharedMemorySize, (int)L_END);
  hipLaunchKernelGGL(fused_decode_kernel, dim3(256), dim3(768), L_END, stream, a);
}
```

```cpp
#include <hip/hip_runtime.h>
#include <stdint.h>
#include <type_traits>

namespace {

#define LAS __attribute__((address_space(3)))
typedef unsigned int u32;
typedef u32 v4u __attribute__((ext_vector_type(4)));
typedef u32 v2u __attribute__((ext_vector_type(2)));
typedef float v4f __attribute__((ext_vector_type(4)));
typedef float v2f __attribute__((ext_vector_type(2)));
typedef short v4s __attribute__((ext_vector_type(4)));
typedef short v8s __attribute__((ext_vector_type(8)));
typedef __bf16 v8b __attribute__((ext_vector_type(8)));
typedef __bf16 v2b __attribute__((ext_vector_type(2)));
typedef __amdgpu_buffer_rsrc_t rsrc_t;

#define NBURST 8
#define WIN 3
#define WIN_W 6
#define WT_W 6
#define WT_M 10
#define RAMP_M 3
#define GATE_M 3
#define GATE_W -1
#define QPOLL 1
#define QSLEEP 2
#define QBX 0
#define QDELAY 0
#define QDEPTH 16
#define RDELAY 0
#define GATE_POLL 1
#define PREFETCH 1
#define CODE_KB 12

constexpr u32 L_WQ = 0, L_K = 18432, L_V = 51200, L_S = 83968, L_FL = 157696;
constexpr u32 L_VEC = L_FL + 192, L_ONE = L_VEC + 1280, L_ST = L_ONE + 1152, L_G = L_ST + 128, L_END = 163840;
constexpr u32 L_S2 = L_S + 36864;
constexpr u32 L_Q = 0, L_SC = 1024, L_P = 2048, L_QS = 8192;
constexpr u32 F_WQ = L_FL, F_K = L_FL + 16, F_V1 = L_FL + 32, F_V2 = L_FL + 48, F_S1 = L_FL + 64, F_S2 = L_FL + 80;
constexpr u32 F_Q = L_FL + 96, F_SC = L_FL + 100, F_X = L_FL + 104, F_O = L_FL + 108, F_T = L_FL + 112, F_QD = L_FL + 120;
static_assert(L_ST + 128 <= L_END && L_P + 8 * 576 <= L_K, "lds");
constexpr u32 WS_EP = 0, WS_QB = 4096, WS_OP = WS_QB + 16 * 4096, OPS = 1152, WS_HP = WS_OP + 256 * OPS;
#define HREP 8
constexpr u32 WS_FP = WS_HP + HREP * 8 * 2304, WS_END = WS_FP + 144 * 2304;
constexpr u32 SPIN_MAX = 1u << 16;

struct Args {
  const void* x; const void* kc; const void* vc; const void* wq; const void* wo; const void* w1; const void* w2;
  void* out; void* ws;
};

template <int N, class F> __device__ __forceinline__ void static_for(F&& f) {
  if constexpr (N > 0) { static_for<N - 1>(f); f(std::integral_constant<int, N - 1>{}); }
}

__device__ __forceinline__ float bf_lo(u32 u) { return __uint_as_float(u << 16); }
__device__ __forceinline__ float bf_hi(u32 u) { return __uint_as_float(u & 0xffff0000u); }
__device__ __forceinline__ u32 pk_bf(float lo, float hi) {
  v2b r = __builtin_convertvector((v2f){lo, hi}, v2b);
  return __builtin_bit_cast(u32, r);
}
__device__ __forceinline__ float rbf(float f) { return bf_lo(pk_bf(f, f)); }

template <int C> __device__ __forceinline__ float dppf(float v) {
  return __builtin_bit_cast(float, __builtin_amdgcn_update_dpp(0, __builtin_bit_cast(int, v), C, 0xf, 0xf, true));
}
__device__ __forceinline__ float r16_sum(float v) { v += dppf<0xB1>(v); v += dppf<0x4E>(v); v += dppf<0x141>(v); v += dppf<0x140>(v); return v; }
__device__ __forceinline__ float r16_max(float v) { v = fmaxf(v, dppf<0xB1>(v)); v = fmaxf(v, dppf<0x4E>(v)); v = fmaxf(v, dppf<0x141>(v)); v = fmaxf(v, dppf<0x140>(v)); return v; }
__device__ __forceinline__ float sw16_add(float a, float b) { auto r = __builtin_amdgcn_permlane16_swap(__float_as_uint(a), __float_as_uint(b), false, false); return __uint_as_float(r[0]) + __uint_as_float(r[1]); }
__device__ __forceinline__ float sw32_add(float a, float b) { auto r = __builtin_amdgcn_permlane32_swap(__float_as_uint(a), __float_as_uint(b), false, false); return __uint_as_float(r[0]) + __uint_as_float(r[1]); }
__device__ __forceinline__ float x16_sum(float v) { return sw16_add(v, v); }
__device__ __forceinline__ float x32_sum(float v) { return sw32_add(v, v); }
__device__ __forceinline__ float x16_max(float v) { auto r = __builtin_amdgcn_permlane16_swap(__float_as_uint(v), __float_as_uint(v), false, false); return fmaxf(__uint_as_float(r[0]), __uint_as_float(r[1])); }
__device__ __forceinline__ float x32_max(float v) { auto r = __builtin_amdgcn_permlane32_swap(__float_as_uint(v), __float_as_uint(v), false, false); return fmaxf(__uint_as_float(r[0]), __uint_as_float(r[1])); }
__device__ __forceinline__ float rs4(v4f a) { return sw32_add(sw16_add(a[0], a[1]), sw16_add(a[2], a[3])); }

template <class T> __device__ __forceinline__ T lds_ld(u32 a) { return *(const LAS T*)(size_t)a; }
template <class T> __device__ __forceinline__ void lds_st(u32 a, T v) { *(LAS T*)(size_t)a = v; }
__device__ __forceinline__ v4s lds_tr(u32 a) { return __builtin_amdgcn_ds_read_tr16_b64_v4i16((LAS v4s*)(size_t)a); }
__device__ __forceinline__ v8b cat8(v4s a, v4s b) { return __builtin_bit_cast(v8b, __builtin_shufflevector(a, b, 0, 1, 2, 3, 4, 5, 6, 7)); }
__device__ __forceinline__ v8b lds_frag(u32 a) { return __builtin_bit_cast(v8b, lds_ld<v4u>(a)); }
__device__ __forceinline__ v4f mfma32(v8b a, v8b b, v4f c) { return __builtin_amdgcn_mfma_f32_16x16x32_bf16(a, b, c, 0, 0, 0); }
__device__ __forceinline__ v4f mfma16(v4s a, v4s b, v4f c) { return __builtin_amdgcn_mfma_f32_16x16x16bf16_1k(a, b, c, 0, 0, 0); }

template <class T> __device__ __forceinline__ void pin(T& v) { asm volatile("" : "+v"(v)); }
__device__ __forceinline__ void lgkm0() { asm volatile("s_waitcnt lgkmcnt(0)" ::: "memory"); }
template <int N> __device__ __forceinline__ void vmwait() { asm volatile("s_waitcnt vmcnt(%0)" ::"n"(N) : "memory"); }
__device__ __forceinline__ void cbar() { asm volatile("" ::: "memory"); }
__device__ __forceinline__ void wg_barrier() { lgkm0(); __builtin_amdgcn_s_barrier(); cbar(); }

__device__ __forceinline__ void dma(rsrc_t r, u32 voff, u32 soff, u32 lds) {
  asm volatile("s_mov_b32 m0, %0\n\ts_nop 0\n\tbuffer_load_dwordx4 %1, %2, %3 offen nt lds" ::"s"(lds), "v"(voff), "s"(r), "s"(soff) : "memory");
}

__device__ __forceinline__ void dma_poll(rsrc_t r, u32 voff, u32 soff, u32 lds) {
  asm volatile("s_mov_b32 m0, %0\n\ts_nop 0\n\tbuffer_load_dwordx4 %1, %2, %3 offen sc1 lds" ::"s"(lds), "v"(voff), "s"(r), "s"(soff) : "memory");
}

__device__ __forceinline__ void set_flag(u32 a, int lane) {
  if (lane == 0) *(volatile LAS u32*)(size_t)a = 1u;
}
template <int SLEEP> __device__ __forceinline__ void wait_flag4(u32 a) {
#pragma unroll 1
  for (u32 it = 0; it < (1u << 22); ++it) {
    v4u v;
    asm volatile("ds_read_b128 %0, %1\n\ts_waitcnt lgkmcnt(0)" : "=v"(v) : "v"(a) : "memory");
    if (__builtin_amdgcn_readfirstlane(v.x & v.y & v.z & v.w)) break;
    if (SLEEP) __builtin_amdgcn_s_sleep(SLEEP);
  }
}
template <int SLEEP> __device__ __forceinline__ void wait_cnt(u32 a, u32 n) {
#pragma unroll 1
  for (u32 it = 0; it < (1u << 22); ++it) {
    u32 v;
    asm volatile("ds_read_b32 %0, %1\n\ts_waitcnt lgkmcnt(0)" : "=v"(v) : "v"(a) : "memory");
    if (__builtin_amdgcn_readfirstlane(v) >= n) break;
    if (SLEEP) __builtin_amdgcn_s_sleep(SLEEP);
  }
}
__device__ __forceinline__ void count_up(u32 a, int lane) {
  if (lane == 0) __hip_atomic_fetch_add((LAS u32*)(size_t)a, 1u, __ATOMIC_RELAXED, __HIP_MEMORY_SCOPE_WORKGROUP);
}

__device__ __forceinline__ v4u gld(rsrc_t r, u32 off) { return __builtin_amdgcn_raw_buffer_load_b128(r, off, 0, 16); }
__device__ __forceinline__ u32 gld1(rsrc_t r, u32 off) { return __builtin_amdgcn_raw_buffer_load_b32(r, off, 0, 16); }
__device__ __forceinline__ void gst(rsrc_t r, u32 off, v4u v) { __builtin_amdgcn_raw_buffer_store_b128(v, r, off, 0, 16); }
__device__ __forceinline__ void gst1(rsrc_t r, u32 off, u32 v) { __builtin_amdgcn_raw_buffer_store_b32(v, r, off, 0, 16); }
__device__ __forceinline__ u32 pack1(float v, u32 t) { return ((__float_as_uint(v) + 0x200u) & ~0x3ffu) | t; }
__device__ __forceinline__ v4u pack4(v4f v, u32 t) { return (v4u){pack1(v[0], t), pack1(v[1], t), pack1(v[2], t), pack1(v[3], t)}; }
__device__ __forceinline__ bool none(u32 bad) { return __all(bad == 0u); }
__device__ __forceinline__ u32 low10(v4u y) { return (y.x | y.y | y.z | y.w) & 0x3ffu; }

template <int N> __device__ __forceinline__ void poll_all(rsrc_t r, u32 off, u32 stride, u32 t, v4f (&y)[N]) {
#pragma unroll 1
  for (u32 it = 0; it < SPIN_MAX; ++it) {
    cbar();
    v4u w[N];
    u32 bad = 0;
    static_for<N>([&](auto i_) { constexpr int i = decltype(i_)::value; w[i] = gld(r, off + i * stride); });
    static_for<N>([&](auto i_) { constexpr int i = decltype(i_)::value; w[i] ^= t; bad |= low10(w[i]); y[i] = __builtin_bit_cast(v4f, w[i]); });
    if (none(bad)) break;
  }
}
template <int N> __device__ __forceinline__ void poll_pend(rsrc_t r, u32 off, u32 stride, u32 t, v4f (&y)[N]) {
  u32 pend = (1u << N) - 1u;
  v4u w[N];
#pragma unroll 1
  for (u32 it = 0; it < SPIN_MAX; ++it) {
    cbar();
    static_for<N>([&](auto i_) { constexpr int i = decltype(i_)::value; if (pend & (1u << i)) w[i] = gld(r, off + i * stride); });
    static_for<N>([&](auto i_) {
      constexpr int i = decltype(i_)::value;
      if (pend & (1u << i)) {
        w[i] ^= t;
        if (none(low10(w[i]))) pend &= ~(1u << i);
      }
    });
    if (pend == 0u) break;
  }
  static_for<N>([&](auto i_) { constexpr int i = decltype(i_)::value; y[i] = __builtin_bit_cast(v4f, w[i]); });
}

#define STAMP(i) do { } while (0)
#define STAMP_DECL do { } while (0)
#define STAMP_FLUSH() do { } while (0)
#define AMP_SPIN() do { } while (0)

__device__ __forceinline__ u32 t128_tr(int lane, int t, int nb) {
  const int g = lane >> 4, q = (lane & 15) >> 2, p = lane & 3;
  const int r = 8 * g + 4 * t + q;
  const int f = (q >> 1) | (((t ^ g) & 1) << 1);
  return (u32)(r * 128 + ((nb ^ f) << 5) + ((p >> 1) << 4) + ((p & 1) << 3));
}
__device__ __forceinline__ u32 t128_tr16(int lane, int nb) {
  const int g = lane >> 4, q = (lane & 15) >> 2, p = lane & 3;
  const int r = 4 * g + q;
  const int f = (q >> 1) | (((g ^ (g >> 1)) & 1) << 1);
  return (u32)(r * 128 + ((nb ^ f) << 5) + ((p >> 1) << 4) + ((p & 1) << 3));
}
__device__ __forceinline__ u32 off_b(u32 row, u32 ch) { return 256u * row + 16u * (ch ^ (((row & 3) << 2) | ((row >> 2) & 3))); }

constexpr int N_WQ = 5, N_K = 13, N_V1 = 17, N_V2 = 21;
template <int WIN_, int NT_, int WT_, int RAMP_, int GATE_> struct Sched {
  static constexpr int NT = NT_, RAMP = RAMP_, WT = WT_, GATE = GATE_;
  static constexpr int win(int n) { return n <= N_V2 + RAMP_ ? WIN_ : (WIN_ + (n - N_V2 - RAMP_) < WT_ ? WIN_ + (n - N_V2 - RAMP_) : WT_); }
  static constexpr int landed(int n) { return n < NBURST ? 0 : (n - win(n) > 0 ? n - win(n) : 0); }
};
template <class C, int n> __device__ __forceinline__ void after(int lane, int lw) {
  if constexpr (n >= NBURST && n > C::win(n) && C::landed(n) > C::landed(n - 1)) vmwait<C::win(n)>();
  constexpr int a = C::landed(n - 1), b = C::landed(n);
  if constexpr (a < N_WQ && b >= N_WQ) set_flag(F_WQ + 4 * lw, lane);
  if constexpr (a < N_K && b >= N_K) set_flag(F_K + 4 * lw, lane);
  if constexpr (a < N_V1 && b >= N_V1) set_flag(F_V1 + 4 * lw, lane);
  if constexpr (a < N_V2 && b >= N_V2) set_flag(F_V2 + 4 * lw, lane);
}

__device__ __forceinline__ u32 load_epoch(const void* ws) {
  return *(const volatile u32*)ws;
}

__global__ void __launch_bounds__(768, 3) fused_decode_kernel(Args a) {
  extern __shared__ __attribute__((aligned(16))) unsigned char smem[];
  const int tid = threadIdx.x, lane = tid & 63;
  const int wv = __builtin_amdgcn_readfirstlane(tid >> 6);
  const int b = blockIdx.x, x = b & 7, j = b >> 3;
  const int g = 2 * x + (j >> 4), s = j & 15;
  const int role = j < 9 ? 0 : (j < 27 ? 1 : 3);
  const int jj = j;
  const int mi = 18 * x + j - 9;
  const int rr = (j - 27) * 8 + x;
  const int g16 = lane >> 4, c16 = lane & 15;
  STAMP_DECL;
  STAMP(0);
  int cw;
  u32 tag32;
  u32 xres = 0;
  v8b B[16];
  v8b B2[4];
  const rsrc_t rws = __builtin_amdgcn_make_buffer_rsrc(a.ws, 0, WS_END, 0x00020000);
  const rsrc_t rx = __builtin_amdgcn_make_buffer_rsrc((void*)a.x, 0, 1152, 0x00020000);

  if (wv < 4) {
    const int lw = wv;
    const u32 vt = (u32)(((lane & 7) ^ (((lane >> 4) & 1) << 1) ^ (((lane >> 5) & 1) << 2)) * 16);
    if (role == 1 && lw == 0) xres = __builtin_amdgcn_raw_buffer_load_b16(rx, (u32)(512 + 4 * c16 + g16) * 2u, 0, 0);
    {
      const rsrc_t rq = __builtin_amdgcn_make_buffer_rsrc((void*)a.wq, 0, 576 * 4096 * 2, 0x00020000);
      const u32 voff = (u32)(lane >> 3) * 8192u + vt;
      const u32 base = (u32)(s & 3) * 144u * 8192u + (u32)(g * 256 + ((s >> 2) ^ QBX) * 64) * 2u;
      static_for<5>([&](auto k_) {
        constexpr int k = decltype(k_)::value;
        const u32 i = (u32)min(lw + 4 * k, 17);
        dma(rq, voff ^ ((i & 1u) << 6), base + i * 65536u, L_WQ + i * 1024u);
      });
    }
    static_assert(NBURST > 5, "no flag may be published before the first barrier");
    __builtin_amdgcn_s_barrier();
    cbar();
    const u32 vkv = ((u32)(lane >> 4) * 256u + (u32)(((lane & 15) ^ ((lane >> 4) << 2)) * 16)) ^ (u32)(lw << 4);
    const u32 bkv = (u32)(g * 8192 + 6144 + s * 128) * 256u + lw * 1024u;
    const rsrc_t rk = __builtin_amdgcn_make_buffer_rsrc((void*)a.kc, 0, 16 * 8192 * 256, 0x00020000);
    const rsrc_t rv = __builtin_amdgcn_make_buffer_rsrc((void*)a.vc, 0, 16 * 8192 * 256, 0x00020000);
    const rsrc_t rw = __builtin_amdgcn_make_buffer_rsrc((void*)(role == 0 ? a.wo : a.w1), 0, role == 0 ? 4096u * 576u * 2u : 4608u * 576u * 2u, 0x00020000);
    const rsrc_t rw2 = __builtin_amdgcn_make_buffer_rsrc((void*)a.w2, 0, 4608u * 576u * 2u, 0x00020000);
    const u32 wbase = role == 0 ? (u32)x * 512u * 1152u + (u32)jj * 128u : (u32)mi * 64u + (u32)lw * 16u * 9216u;
    const u32 wvoff = role == 0 ? ((u32)(lane >> 3) * 1152u + vt) ^ (u32)((lw & 1) << 6)
                                : (u32)(lane >> 2) * 9216u + (u32)(((lane & 3) ^ (lane >> 5) ^ ((lw & 1) << 1)) * 16);
    const u32 w2base = (u32)mi * 36864u + (u32)lw * 9216u, w2voff = (u32)(lane * 16) ^ (u32)(lw << 5);
    auto stream = [&](auto c_) {
      using C = decltype(c_);
      static_for<8>([&](auto k_) {
        constexpr int k = decltype(k_)::value;
        dma(rk, vkv, bkv + k * 4096u, L_K + lw * 1024u + k * 4096u);
        after<C, N_WQ + k + 1>(lane, lw);
      });
      STAMP(1);
      static_for<8>([&](auto k_) {
        constexpr int k = decltype(k_)::value;
        dma(rv, vkv, bkv + k * 4096u, L_V + lw * 1024u + k * 4096u);
        after<C, N_K + k + 1>(lane, lw);
      });
      STAMP(2);
      if constexpr (C::NT == 0) {
        if constexpr (C::landed(N_V2) < N_V1) { vmwait<4>(); set_flag(F_V1 + 4 * lw, lane); }
        vmwait<0>();
        set_flag(F_V2 + 4 * lw, lane);
      } else {
        static_assert(C::landed(N_V2 + C::NT) >= N_V2, "V flags must be published inside the tile phase");
        static_for<C::NT>([&](auto k_) {
          constexpr int k = decltype(k_)::value;
          const u32 i = lw + 4 * k;
          if constexpr (C::GATE == k) {
            vmwait<k>();
            set_flag(F_V1 + 4 * lw, lane);
            set_flag(F_V2 + 4 * lw, lane);
            wait_cnt<1>(F_O, 8);
          }
          if constexpr (C::NT == 16) dma(rw, wvoff, wbase + i * (8u * 1152u), L_S + i * 1024u);
          else if constexpr (k < 9) dma(rw, wvoff, wbase + k * (64u * 9216u), L_S + i * 1024u);
          else dma(rw2, w2voff, w2base + (k - 9) * 1024u, L_S2 + lw * 9216u + (k - 9) * 1024u);
          after<C, N_V2 + k + 1>(lane, lw);
        });
        constexpr int NH = C::NT / 2;
        vmwait<(C::NT - NH < C::win(N_V2 + C::NT) ? C::NT - NH : C::win(N_V2 + C::NT))>();
        set_flag(F_S1 + 4 * lw, lane);
        wait_flag4<1>(F_S1);
        if constexpr (C::NT == 16) {
          const u32 a0 = L_S + t128_tr(lane, 0, lw), a1 = L_S + t128_tr(lane, 1, lw);
#pragma unroll
          for (int c = 0; c < NH; ++c) B[c] = cat8(lds_tr(a0 + c * 4096), lds_tr(a1 + c * 4096));
          vmwait<0>();
          set_flag(F_S2 + 4 * lw, lane);
          STAMP(3);
          wait_flag4<1>(F_S2);
#pragma unroll
          for (int c = NH; c < C::NT; ++c) B[c] = cat8(lds_tr(a0 + c * 4096), lds_tr(a1 + c * 4096));
        } else {
          const u32 a0 = L_S + (u32)(((lane >> 1) & 1) * 288 + 8 * g16 + (c16 >> 2)) * 64u + (u32)((lw ^ g16) << 4) + (u32)(lane & 1) * 8u;
#pragma unroll
          for (int c = 0; c < 9; ++c) B[c] = cat8(lds_tr(a0 + c * 2048), lds_tr(a0 + 256 + c * 2048));
          vmwait<0>();
          set_flag(F_S2 + 4 * lw, lane);
          STAMP(3);
        }
      }
    };
    if (role == 0) stream(Sched<WIN_W, 16, WT_W, 0, GATE_W>{});
    else if (role == 1) stream(Sched<WIN, 18, WT_M, RAMP_M, GATE_M>{});
    else { stream(Sched<WIN, 0, WIN, 0, -1>{}); if (lw != 0 || rr >= 36) { STAMP_FLUSH(); return; } }
    wait_cnt<1>(F_T, 1);
    tag32 = lds_ld<u32>(F_T + 4);
    lgkm0();
    STAMP(4);
    cw = (lw == 0 && role != 0) ? 8 : -1;
  } else {
    cw = wv - 4;
    v4u xa; v2u xb, xq;
    if (cw == 0) {
      xa = __builtin_amdgcn_raw_buffer_load_b128(rx, lane * 24, 0, 0);
      xb = __builtin_amdgcn_raw_buffer_load_b64(rx, lane * 24 + 16, 0, 0);
      xq = __builtin_amdgcn_raw_buffer_load_b64(rx, (s & 3) * 288 + lane * 8, 0, 0);
    }
    const u32 ep = __builtin_amdgcn_raw_buffer_load_b32(rws, WS_EP, 0, 0);
    if (role == 1) xres = __builtin_amdgcn_raw_buffer_load_b16(rx, (u32)(64 * cw + 4 * c16 + g16) * 2u, 0, 0);
    u32 keep = 0;
    if (role == 3 && rr < 36 && cw == 0) {
      xres = __builtin_amdgcn_raw_buffer_load_b16(rx, (u32)(16 * rr + 4 * (c16 & 3) + g16) * 2u, 0, 0);
      keep = __builtin_amdgcn_raw_buffer_load_b32(__builtin_amdgcn_make_buffer_rsrc(a.out, 0, 1152, 0x00020000), 0, 0, 0);
    }
    if (cw == 7) {
      if (lane < 48) lds_st<u32>(L_FL + lane * 4, 0u);
      if (lane < 32) lds_st<u32>(L_ST + lane * 4, 0u);
      lds_st<v4u>(L_ONE + lane * 16, (v4u){0x3f803f80u, 0x3f803f80u, 0x3f803f80u, 0x3f803f80u});
      if (lane < 8) lds_st<v4u>(L_ONE + 1024 + lane * 16, (v4u){0x3f803f80u, 0x3f803f80u, 0x3f803f80u, 0x3f803f80u});
    }
    wg_barrier();
    if (cw == 0) {
      float v[12];
#pragma unroll
      for (int i = 0; i < 4; ++i) { v[2 * i] = bf_lo(xa[i]); v[2 * i + 1] = bf_hi(xa[i]); }
#pragma unroll
      for (int i = 0; i < 2; ++i) { v[8 + 2 * i] = bf_lo(xb[i]); v[9 + 2 * i] = bf_hi(xb[i]); }
      const float x0 = __uint_as_float(__builtin_amdgcn_readfirstlane(__float_as_uint(v[0])));
      float s1 = 0.f, s2 = 0.f;
#pragma unroll
      for (int i = 0; i < 12; ++i) { const float d = v[i] - x0; s1 += d; s2 += d * d; }
      if (lane >= 48) { s1 = 0.f; s2 = 0.f; }
      const float sr = r16_sum(x16_sum(sw32_add(s1, s2)));
      auto r2 = __builtin_amdgcn_permlane32_swap(__float_as_uint(sr), __float_as_uint(sr), false, false);
      const float md = __uint_as_float(r2[0]) * (1.0f / 576.0f);
      const float var = fmaxf(__uint_as_float(r2[1]) * (1.0f / 576.0f) - md * md, 0.f);
      const float m = rbf(x0 + md);
      const float den = rbf(__builtin_amdgcn_sqrtf(rbf(rbf(var) + 1e-5f)));
      const float rden = __builtin_amdgcn_rcpf(den);
      v2u n;
#pragma unroll
      for (int i = 0; i < 2; ++i) {
        const u32 d = pk_bf(bf_lo(xq[i]) - m, bf_hi(xq[i]) - m);
        n[i] = pk_bf(bf_lo(d) * rden, bf_hi(d) * rden);
      }
      if (lane < 36) lds_st<v2u>(L_VEC + lane * 8, n);
      lgkm0();
      set_flag(F_X, lane);
      STAMP(1);
    }
    tag32 = __builtin_amdgcn_readfirstlane(ep) + 1u;
    const u32 t10 = tag32 & 0x3ffu;
    if (cw < 4) {
      const u32 b0 = L_WQ + t128_tr(lane, 0, cw), b1 = L_WQ + t128_tr(lane, 1, cw), b2 = L_WQ + 16384 + t128_tr16(lane, cw);
      wait_cnt<1>(F_X, 1);
      v8b A[4];
#pragma unroll
      for (int c = 0; c < 4; ++c) A[c] = lds_frag(L_VEC + c * 64 + g16 * 16);
      const v4s A4 = lds_ld<v4s>(L_VEC + 256 + g16 * 8);
      wait_flag4<1>(F_WQ);
      STAMP(2);
      v4f acc = {0.f, 0.f, 0.f, 0.f};
#pragma unroll
      for (int c = 0; c < 4; ++c) acc = mfma32(A[c], cat8(lds_tr(b0 + c * 4096), lds_tr(b1 + c * 4096)), acc);
      acc = mfma16(A4, lds_tr(b2), acc);
      if (lane < 16) gst1(rws, WS_QB + g * 4096u + (u32)(s & 3) * 1024u + (u32)(((s >> 2) ^ QBX) * 64 + cw * 16 + c16) * 4u, pack1(acc[0], t10));
      if (QPOLL) count_up(F_QD, lane);
      STAMP(3);
    }
    if (cw == 7) { if (lane == 0) lds_st<v2u>(F_T, (v2u){1u, tag32}); }
    if (cw == 4) {
      v4f y[4];
      if (QPOLL) {
        wait_cnt<1>(F_QD, 4);
        const u32 sl = L_QS + lane * 16u, inv = t10 ^ 0x3ffu;
        static_for<4>([&](auto i_) { constexpr int i = decltype(i_)::value; lds_st<v4u>(sl + i * 1024u, (v4u){inv, inv, inv, inv}); });
        lgkm0();
        u32 pend = 15u;
        if (QDELAY) __builtin_amdgcn_s_sleep(QDELAY);
        STAMP(8);
#pragma unroll 1
        for (u32 it = 0; it < SPIN_MAX; ++it) {
          static_for<4>([&](auto i_) { constexpr int i = decltype(i_)::value; if (pend & (1u << i)) dma_poll(rws, lane * 16u, WS_QB + g * 4096u + i * 1024u, L_QS + i * 1024u); });
          if (QSLEEP) __builtin_amdgcn_s_sleep(QSLEEP);
          vmwait<QDEPTH>();
          v4u w[4];
          asm volatile("ds_read_b128 %0, %4\n\tds_read_b128 %1, %4 offset:1024\n\tds_read_b128 %2, %4 offset:2048\n\tds_read_b128 %3, %4 offset:3072\n\ts_waitcnt lgkmcnt(0)"
                       : "=&v"(w[0]), "=&v"(w[1]), "=&v"(w[2]), "=&v"(w[3]) : "v"(sl) : "memory");
          static_for<4>([&](auto i_) {
            constexpr int i = decltype(i_)::value;
            w[i] ^= t10;
            y[i] = __builtin_bit_cast(v4f, w[i]);
            if (none(low10(w[i]))) pend &= ~(1u << i);
          });
          if (pend == 0u) break;
        }
      } else {
        wait_flag4<1>(F_WQ);
        poll_all<4>(rws, WS_QB + g * 4096u + lane * 16u, 1024u, t10, y);
      }
      const v4f q = (y[0] + y[1]) + (y[2] + y[3]);
      lds_st<v2u>(L_Q + (lane >> 5) * 272 + (lane & 31) * 8, (v2u){pk_bf(q[0], q[1]), pk_bf(q[2], q[3])});
      lgkm0();
      set_flag(F_Q, lane);
      STAMP(3);
    }
    wait_flag4<1>(F_K);
    v8b KA[4];
#pragma unroll
    for (int c = 0; c < 4; ++c) KA[c] = lds_frag(L_K + off_b(16 * cw + c16, 4 * c + g16));
    wait_cnt<1>(F_Q, 1);
    STAMP(4);
    {
      v4f acc = {0.f, 0.f, 0.f, 0.f};
#pragma unroll
      for (int c = 0; c < 4; ++c) acc = mfma32(KA[c], lds_frag(L_Q + (lane & 1) * 272 + (32 * c + 8 * g16) * 2), acc);
      v4f sc;
#pragma unroll
      for (int i = 0; i < 4; ++i) sc[i] = rbf(rbf(acc[i]) * 0.08838834764831845f);
      if (c16 < 2) lds_st<v4f>(L_SC + c16 * 512 + (16 * cw + 4 * g16) * 4, sc);
      lgkm0();
      count_up(F_SC, lane);
      wait_cnt<0>(F_SC, 8);
    }
    STAMP(5);
    {
      const v4f sc = lds_ld<v4f>(L_SC + (lane >> 5) * 512 + (lane & 31) * 16);
      float m = fmaxf(fmaxf(sc[0], sc[1]), fmaxf(sc[2], sc[3]));
      m = x16_max(r16_max(m));
      v4f e;
#pragma unroll
      for (int i = 0; i < 4; ++i) e[i] = __builtin_amdgcn_exp2f((sc[i] - m) * 1.4426950408889634f);
      float l = (e[0] + e[1]) + (e[2] + e[3]);
      l = x16_sum(r16_sum(l));
      lds_st<v2u>(L_P + cw * 576 + (lane >> 5) * 272 + (lane & 31) * 8, (v2u){pk_bf(e[0], e[1]), pk_bf(e[2], e[3])});
      if (cw == 0) {
        const float m1 = __uint_as_float(__builtin_amdgcn_readlane(__float_as_uint(m), 32)), l1 = __uint_as_float(__builtin_amdgcn_readlane(__float_as_uint(l), 32));
        if (lane == 0) gst(rws, WS_OP + (g * 16 + s) * OPS + 1024, pack4((v4f){m, l, m1, l1}, t10));
      }
    }
    v8b PB[4];
#pragma unroll
    for (int c = 0; c < 4; ++c) PB[c] = lds_frag(L_P + cw * 576 + (lane & 1) * 272 + (32 * c + 8 * g16) * 2);
    {
      const int q = c16 >> 2, p = lane & 3;
      v4f acc = {0.f, 0.f, 0.f, 0.f};
      wait_flag4<1>(F_V1);
#pragma unroll
      for (int c = 0; c < 2; ++c) {
        const u32 r0 = 32 * c + 8 * g16 + q;
        acc = mfma32(cat8(lds_tr(L_V + off_b(r0, 2 * cw + (p >> 1)) + 8 * (p & 1)), lds_tr(L_V + off_b(r0 + 4, 2 * cw + (p >> 1)) + 8 * (p & 1))), PB[c], acc);
      }
      wait_flag4<0>(F_V2);
      STAMP(6);
#pragma unroll
      for (int c = 2; c < 4; ++c) {
        const u32 r0 = 32 * c + 8 * g16 + q;
        acc = mfma32(cat8(lds_tr(L_V + off_b(r0, 2 * cw + (p >> 1)) + 8 * (p & 1)), lds_tr(L_V + off_b(r0 + 4, 2 * cw + (p >> 1)) + 8 * (p & 1))), PB[c], acc);
      }
      if (c16 < 2) gst(rws, WS_OP + (g * 16 + s) * OPS + c16 * 512 + (16 * cw + 4 * g16) * 4, pack4(acc, t10));
      count_up(F_O, lane);
    }
    STAMP(7);
    asm volatile("" ::"v"(keep));
    if (role == 3 && rr >= 36) { STAMP_FLUSH(); return; }
  }

  const u32 t10 = tag32 & 0x3ffu;
  if (role == 3) {
    auto total = [](v4f v) { float r = rs4(v); r += dppf<0x124>(r); r += dppf<0x128>(r); return r; };
    const u32 col = (u32)(4 * (c16 & 3) + g16);
    u32 wa = L_VEC + col * 48u + (u32)cw * 4u, ra = L_VEC + col * 48u, fa = WS_FP + (u32)rr * 9216u + (u32)cw * 1024u + (u32)lane * 16u;
    u32 oa = c16 < 4 ? (u32)(16 * rr + col) * 2u : 0x7ffffff0u;
    pin(wa); pin(ra); pin(fa); pin(oa);
    const rsrc_t ro = __builtin_amdgcn_make_buffer_rsrc(a.out, 0, 1152, 0x00020000);
    float h = 0.f;
    if (cw == 0) {
      const u32 ho = lane < 32 ? WS_HP + (u32)x * (8u * 2304u) + (u32)(lane >> 2) * 2304u + (u32)(16 * rr + 4 * (lane & 3)) * 4u : 0x7ffffff0u;
      const u32 msk = lane < 32 ? 0x3ffu : 0u, tx = lane < 32 ? t10 : 0u;
      v4u w;
#pragma unroll 1
      for (u32 it = 0; it < SPIN_MAX; ++it) {
        cbar();
        w = gld(rws, ho) ^ tx;
        if (none((w.x | w.y | w.z | w.w) & msk)) break;
      }
      h = rbf(bf_lo(xres) + rbf(total(__builtin_bit_cast(v4f, w))));
      pin(h);
      STAMP(8);
    }
    if (RDELAY) __builtin_amdgcn_s_sleep(RDELAY);
    v4f y[1];
    poll_all<1>(rws, fa, 0u, t10, y);
    STAMP(9);
    const float part = total(y[0]);
    if (c16 < 4) lds_st<float>(wa, part);
    wg_barrier();
    if (cw != 0) { STAMP_FLUSH(); return; }
    const v4f p0 = lds_ld<v4f>(ra), p1 = lds_ld<v4f>(ra + 16);
    const float p2 = lds_ld<float>(ra + 32);
    const v4f ps = p0 + p1;
    const u32 o = pk_bf(h + rbf(((ps[0] + ps[1]) + (ps[2] + ps[3])) + p2), 0.f);
    __builtin_amdgcn_raw_buffer_store_b16((unsigned short)o, ro, oa, 0, 0);
    if (rr == 0 && lane == 0) gst1(rws, WS_EP, tag32);
    STAMP(10);
    STAMP_FLUSH();
    if (rr == 0) AMP_SPIN();
    return;
  }
  if (cw >= 0) {
    const u32 va = L_VEC + (u32)(64 * cw + 4 * c16 + g16) * 2u;
    if (role == 0) {
      const int gg = 2 * x + (cw >> 2), hh = (cw >> 1) & 1;
      const u32 ob = WS_OP + (u32)(gg * 16 + 4 * g16) * OPS;
      v2u st;
#pragma unroll 1
      for (u32 it = 0; it < SPIN_MAX; ++it) {
        cbar();
        st = __builtin_amdgcn_raw_buffer_load_b64(rws, ob + (u32)(c16 & 3) * OPS + 1024u + hh * 8u, 0, 16);
        st ^= t10;
        if (none((st.x | st.y) & 0x3ffu)) break;
      }
      const float m = __uint_as_float(st.x), l = __uint_as_float(st.y);
      float M = fmaxf(m, dppf<0xB1>(m));
      M = fmaxf(M, dppf<0x4E>(M));
      M = x32_max(x16_max(M));
      const float e = __builtin_amdgcn_exp2f((m - M) * 1.4426950408889634f);
      float L = e * l;
      L += dppf<0xB1>(L); L += dppf<0x4E>(L);
      L = x32_sum(x16_sum(L));
      const float f = e * __builtin_amdgcn_rcpf(L);
      const float fq[4] = {dppf<0x00>(f), dppf<0x55>(f), dppf<0xAA>(f), dppf<0xFF>(f)};
      STAMP(8);
      if (GATE_POLL) wait_flag4<1>(F_S2);
      v4f y[4];
      poll_pend<4>(rws, ob + hh * 512u + (cw & 1) * 256u + c16 * 16u, OPS, t10, y);
      STAMP(9);
      const float o = rs4((fq[0] * y[0] + fq[1] * y[1]) + (fq[2] * y[2] + fq[3] * y[3]));
      lds_st<unsigned short>(va, (unsigned short)pk_bf(o, o));
    } else {
      wait_flag4<1>(F_S2);
      {
        const u32 a0 = L_S2 + (u32)(8 * g16 + (c16 >> 2)) * 1152u + (u32)cw * 128u + (u32)(lane & 3) * 8u;
#pragma unroll
        for (int c = 0; c < 4; ++c) B2[c] = cat8(lds_tr(a0 + (u32)((c ^ g16) * 32)), lds_tr(a0 + 4 * 1152 + (u32)((c ^ g16) * 32)));
        asm volatile("" : "+v"(B2[0]), "+v"(B2[1]), "+v"(B2[2]), "+v"(B2[3]));
      }
      v4f y[2];
      poll_all<2>(rws, WS_HP + (u32)x * (8u * 2304u) + (u32)g16 * 2304u + (u32)(64 * cw + 4 * c16) * 4u, 4 * 2304u, t10, y);
      STAMP(9);
      const float pre = bf_lo(xres) + rbf(rs4(y[0] + y[1]));
      const u32 hb = pk_bf(pre, pre);
      lds_st<unsigned short>(va, (unsigned short)hb);
      const float h = bf_lo(hb);
      const float sr = r16_sum(x16_sum(sw32_add(h, h * h)));
      if ((lane & 31) == 0) lds_st<float>(L_ST + (lane >> 5) * 64 + cw * 4, sr);
    }
  }
  auto mlp_out = [&]() {
    u32 ga = L_G + (u32)g16 * 16u, fa = WS_FP + (u32)(4 * cw + g16) * 9216u + (u32)mi * 64u + (u32)c16 * 4u;
    pin(ga); pin(fa);
    wg_barrier();
    STAMP(12);
    const v8b A = lds_frag(ga);
    const v4f z = {0.f, 0.f, 0.f, 0.f};
    const v4f d0 = mfma32(A, B2[0], z), d1 = mfma32(A, B2[1], z), d2 = mfma32(A, B2[2], z), d3 = mfma32(A, B2[3], z);
    const float v = g16 == 0 ? d0[0] : (g16 == 1 ? d1[0] : (g16 == 2 ? d2[0] : d3[0]));
    gst1(rws, fa, pack1(v, t10));
  };
  if (wv >= 4) {
    wg_barrier();
    STAMP(10);
    if (role == 1) { mlp_out(); STAMP(11); }
    STAMP_FLUSH();
    return;
  }

  {
    const u32 col = (u32)(64 * jj + 16 * wv + c16);
    u32 ab = role == 1 ? ((c16 & 2) ? L_ONE : L_VEC) + (u32)(c16 & 1) * 576u + (u32)g16 * 16u : L_VEC + (u32)g16 * 16u;
    u32 oaddr = role == 1 ? L_G + (u32)(8 * wv + (lane & 7)) * 2u : WS_HP + x * 2304u + col * 4u + (u32)g16 * (8u * 2304u);
    static_assert(HREP == 8, "h is published in eight copies");
    u32 sa = L_ST + (u32)(lane & 31) * 4u;
    asm volatile("" : "+v"(ab), "+v"(oaddr), "+v"(sa));
    v4f acc;
    v4u A0, A1, A2, A3, A4, A5, A6, A7, A8;
#define RD(r, o) "ds_read_b128 %[a" #r "], %[ab] offset:" #o "\n\t"
#define WT(n) "s_waitcnt lgkmcnt(" #n ")\n\t"
#define MF0(r, b) "v_mfma_f32_16x16x32_bf16 %[acc], %[a" #r "], %[b" #b "], 0\n\t"
#define MF(r, b) "v_mfma_f32_16x16x32_bf16 %[acc], %[a" #r "], %[b" #b "], %[acc]\n\t"
#define CH_OUT [acc] "=&v"(acc), [a0] "=&v"(A0), [a1] "=&v"(A1), [a2] "=&v"(A2), [a3] "=&v"(A3), [a4] "=&v"(A4), [a5] "=&v"(A5), [a6] "=&v"(A6)
#define CH_IN16 [ab] "v"(ab), [b0] "v"(B[0]), [b1] "v"(B[1]), [b2] "v"(B[2]), [b3] "v"(B[3]), [b4] "v"(B[4]), [b5] "v"(B[5]), [b6] "v"(B[6]), \
                [b7] "v"(B[7]), [b8] "v"(B[8]), [b9] "v"(B[9]), [b10] "v"(B[10]), [b11] "v"(B[11]), [b12] "v"(B[12]), [b13] "v"(B[13]), \
                [b14] "v"(B[14]), [b15] "v"(B[15])
#define CH_IN18 CH_IN16, [b16] "v"(B[16]), [b17] "v"(B[17])
    if (role == 0) {
      wg_barrier();
      STAMP(10);
      asm volatile(""
                   RD(0, 0) RD(1, 64) RD(2, 128) RD(3, 192) RD(4, 256) RD(5, 320) RD(6, 384) WT(6) MF0(0, 0) RD(0, 448) WT(6)
                   MF(1, 1) RD(1, 512) WT(6) MF(2, 2) RD(2, 576) WT(6) MF(3, 3) RD(3, 640) WT(6) MF(4, 4) RD(4, 704) WT(6)
                   MF(5, 5) RD(5, 768) WT(6) MF(6, 6) RD(6, 832) WT(6) MF(0, 7) RD(0, 896) WT(6) MF(1, 8) RD(1, 960) WT(6)
                   MF(2, 9) WT(5) MF(3, 10) WT(4) MF(4, 11) WT(3) MF(5, 12) WT(2) MF(6, 13) WT(1) MF(0, 14) WT(0) MF(1, 15)
                   "s_nop 7"
                   : CH_OUT : CH_IN16 : "memory");
      gst1(rws, oaddr, pack1(acc[0], t10));
      gst1(rws, oaddr + 4u * 8u * 2304u, pack1(acc[0], t10));
    } else {
      float mean, rden;
      wg_barrier();
      STAMP(10);
      asm volatile("ds_read_b32 %[mean], %[sa]\n\t"
                   RD(0, 0) RD(1, 64) RD(2, 128) RD(3, 192) RD(4, 256) RD(5, 320) RD(6, 384) RD(7, 448) RD(8, 512)
                   "s_waitcnt lgkmcnt(9)\n\t"
                   "v_add_f32_dpp %[mean], %[mean], %[mean] quad_perm:[1,0,3,2] row_mask:0xf bank_mask:0xf bound_ctrl:1\n\t"
                   WT(8) MF0(0, 0)
                   "v_add_f32_dpp %[mean], %[mean], %[mean] quad_perm:[2,3,0,1] row_mask:0xf bank_mask:0xf bound_ctrl:1\n\t"
                   WT(7) MF(1, 1)
                   "v_add_f32_dpp %[mean], %[mean], %[mean] row_half_mirror row_mask:0xf bank_mask:0xf bound_ctrl:1\n\t"
                   WT(6) MF(2, 2)
                   "v_add_f32_dpp %[mean], %[mean], %[mean] row_mirror row_mask:0xf bank_mask:0xf bound_ctrl:1\n\t"
                   "v_mov_b32 %[rden], %[mean]\n\t"
                   WT(5) MF(3, 3)
                   "v_permlane16_swap_b32 %[mean], %[rden]\n\t"
                   "v_mul_f32 %[mean], 0x3ae38e39, %[mean]\n\t"
                   "v_mul_f32 %[rden], 0x3ae38e39, %[rden]\n\t"
                   WT(4) MF(4, 4)
                   "v_fma_f32 %[rden], -%[mean], %[mean], %[rden]\n\t"
                   "v_max_f32 %[rden], 0, %[rden]\n\t"
                   "v_cvt_pk_bf16_f32 %[rden], %[rden], %[rden]\n\t"
                   WT(3) MF(5, 5)
                   "v_lshlrev_b32 %[rden], 16, %[rden]\n\t"
                   "v_add_f32 %[rden], 0x3727c5ac, %[rden]\n\t"
                   "v_cvt_pk_bf16_f32 %[rden], %[rden], %[rden]\n\t"
                   WT(2) MF(6, 6)
                   "v_lshlrev_b32 %[rden], 16, %[rden]\n\t"
                   "v_sqrt_f32 %[rden], %[rden]\n\t"
                   "v_cvt_pk_bf16_f32 %[mean], %[mean], %[mean]\n\t"
                   WT(1) MF(7, 7)
                   "v_cvt_pk_bf16_f32 %[rden], %[rden], %[rden]\n\t"
                   "v_lshlrev_b32 %[mean], 16, %[mean]\n\t"
                   "v_lshlrev_b32 %[rden], 16, %[rden]\n\t"
                   WT(0) MF(8, 8)
                   "v_rcp_f32 %[rden], %[rden]\n\t"
                   "s_nop 7"
                   : CH_OUT, [a7] "=&v"(A7), [a8] "=&v"(A8), [mean] "=&v"(mean), [rden] "=&v"(rden)
                   : [ab] "v"(ab), [b0] "v"(B[0]), [b1] "v"(B[1]), [b2] "v"(B[2]), [b3] "v"(B[3]), [b4] "v"(B[4]), [b5] "v"(B[5]),
                     [b6] "v"(B[6]), [b7] "v"(B[7]), [b8] "v"(B[8]), [sa] "v"(sa) : "memory");
      const float dot = acc[0] + dppf<0x128>(acc[1]), cs = acc[2] + dppf<0x128>(acc[3]);
      const float pre = rbf((dot - mean * cs) * rden);
      const float gv = pre * __builtin_amdgcn_rcpf(1.0f + __builtin_amdgcn_exp2f(pre * -1.4426950408889634f));
      if (lane < 8) lds_st<unsigned short>(oaddr, (unsigned short)pk_bf(gv, gv));
      if (wv == 0) mlp_out(); else wg_barrier();
    }
    STAMP(11);
    STAMP_FLUSH();
  }
}

}

extern "C" void kernel_launch(void* const* d_in, const int* in_sizes, int n_in, void* d_out, int out_size, void* d_ws, size_t ws_size,
                              hipStream_t stream) {
  (void)in_sizes; (void)n_in; (void)out_size; (void)ws_size;
  Args a;
  a.x = d_in[0]; a.kc = d_in[1]; a.vc = d_in[2]; a.wq = d_in[3]; a.wo = d_in[4]; a.w1 = d_in[6]; a.w2 = d_in[7];
  a.out = d_out; a.ws = d_ws;
  (void)hipFuncSetAttribute((const void*)fused_decode_kernel, hipFuncAttributeMaxDynamicSharedMemorySize, (int)L_END);
  hipLaunchKernelGGL(fused_decode_kernel, dim3(256), dim3(768), L_END, stream, a);
}
```

```cpp
#include <hip/hip_runtime.h>
#include <stdint.h>
#include <type_traits>

namespace {

#define LAS __attribute__((address_space(3)))
typedef unsigned int u32;
typedef unsigned long long u64;
typedef u32 v4u __attribute__((ext_vector_type(4)));
typedef u32 v2u __attribute__((ext_vector_type(2)));
typedef u32 v16u __attribute__((ext_vector_type(16)));
typedef float v4f __attribute__((ext_vector_type(4)));
typedef float v2f __attribute__((ext_vector_type(2)));
typedef short v4s __attribute__((ext_vector_type(4)));
typedef short v8s __attribute__((ext_vector_type(8)));
typedef __bf16 v8b __attribute__((ext_vector_type(8)));
typedef __bf16 v2b __attribute__((ext_vector_type(2)));
typedef __amdgpu_buffer_rsrc_t rsrc_t;

#define NBURST 8
#define WIN 3
#define WIN_W 6
#define WT_W 16
#define WT_M 10
#define RAMP_M 3
#define GATE_M 3
#define GATE_O 8
#define MGK 0
#define GATE_W -1
#define MLAG 4
#define QDELAY 20
#define KALIGN 256
#define LSLEEP 0

constexpr u32 L_WQ = 0, L_K = 18432, L_V = 51200, L_S = 83968, L_FL = 157696;
constexpr u32 L_VEC = L_FL + 192, L_ONE = L_VEC + 1280, L_ST = L_ONE + 1152, L_G = L_ST + 128, L_END = 163840;
constexpr u32 L_S2 = L_S + 36864;
constexpr u32 L_Q = 0, L_SC = 1024;
constexpr u32 F_WQ = L_FL, F_K0 = L_FL + 4, F_K = L_FL + 8, F_S3 = L_FL + 12, F_V1 = L_FL + 16, F_V2 = L_FL + 20, F_S1 = L_FL + 24, F_S2 = L_FL + 28;
constexpr u32 F_Q = L_FL + 32, F_SC = L_FL + 36, F_X = L_FL + 40, F_O = L_FL + 44, F_QD = L_FL + 48;
static_assert(L_G + 64 <= L_END && L_SC + 1024 <= L_K, "lds");
constexpr u32 WS_EP = 0, WS_QB = 4096, WS_OP = WS_QB + 16 * 4096, OPS = 1152, WS_HP = WS_OP + 256 * OPS;
constexpr u32 WS_FP = WS_HP + 8 * 8 * 2304, WS_END = WS_FP + 144 * 2304;
constexpr u32 SPIN_MAX = 1u << 16;
constexpr u32 OOB = 0x7ffffff0u;

struct Args {
  const void* x; const void* kc; const void* vc; const void* wq; const void* wo; const void* w1; const void* w2;
  void* out; void* ws;
};

template <int N, class F> __device__ __forceinline__ void static_for(F&& f) {
  if constexpr (N > 0) { static_for<N - 1>(f); f(std::integral_constant<int, N - 1>{}); }
}

__device__ __forceinline__ float bf_lo(u32 u) { return __uint_as_float(u << 16); }
__device__ __forceinline__ float bf_hi(u32 u) { return __uint_as_float(u & 0xffff0000u); }
__device__ __forceinline__ u32 pk_bf(float lo, float hi) {
  v2b r = __builtin_convertvector((v2f){lo, hi}, v2b);
  return __builtin_bit_cast(u32, r);
}
__device__ __forceinline__ float rbf(float f) { return bf_lo(pk_bf(f, f)); }

template <int C> __device__ __forceinline__ u32 dppu(u32 v) { return (u32)__builtin_amdgcn_update_dpp(0, (int)v, C, 0xf, 0xf, true); }
template <int C> __device__ __forceinline__ float dppf(float v) { return __uint_as_float(dppu<C>(__float_as_uint(v))); }
__device__ __forceinline__ float r16_sum(float v) { v += dppf<0xB1>(v); v += dppf<0x4E>(v); v += dppf<0x141>(v); v += dppf<0x140>(v); return v; }
__device__ __forceinline__ float sw16_add(float a, float b) { auto r = __builtin_amdgcn_permlane16_swap(__float_as_uint(a), __float_as_uint(b), false, false); return __uint_as_float(r[0]) + __uint_as_float(r[1]); }
__device__ __forceinline__ float sw32_add(float a, float b) { auto r = __builtin_amdgcn_permlane32_swap(__float_as_uint(a), __float_as_uint(b), false, false); return __uint_as_float(r[0]) + __uint_as_float(r[1]); }
__device__ __forceinline__ float x16_sum(float v) { return sw16_add(v, v); }
__device__ __forceinline__ float x32_sum(float v) { return sw32_add(v, v); }
__device__ __forceinline__ float vmax(float a, float b) { float r; asm("v_max_f32 %0, %1, %2" : "=v"(r) : "v"(a), "v"(b)); return r; }
#define MAX_DPP(r, v, ctrl) asm volatile("v_max_f32_dpp %0, %1, %1 " ctrl " row_mask:0xf bank_mask:0xf" : "=v"(r) : "v"(v) : "memory")
__device__ __forceinline__ float rs4(v4f a) { return sw32_add(sw16_add(a[0], a[1]), sw16_add(a[2], a[3])); }
__device__ __forceinline__ u32 bfi(u32 m, u32 a1, u32 a0) { return (m & a1) | (~m & a0); }

template <class T> __device__ __forceinline__ T lds_ld(u32 a) { return *(const LAS T*)(size_t)a; }
template <class T> __device__ __forceinline__ void lds_st(u32 a, T v) { *(LAS T*)(size_t)a = v; }
__device__ __forceinline__ v4s lds_tr(u32 a) { return __builtin_amdgcn_ds_read_tr16_b64_v4i16((LAS v4s*)(size_t)a); }
__device__ __forceinline__ v8b cat8(v4s a, v4s b) { return __builtin_bit_cast(v8b, __builtin_shufflevector(a, b, 0, 1, 2, 3, 4, 5, 6, 7)); }
__device__ __forceinline__ v8b lds_frag(u32 a) { return __builtin_bit_cast(v8b, lds_ld<v4u>(a)); }
__device__ __forceinline__ v4f mfma32(v8b a, v8b b, v4f c) { return __builtin_amdgcn_mfma_f32_16x16x32_bf16(a, b, c, 0, 0, 0); }
__device__ __forceinline__ v4f mfma16(v4s a, v4s b, v4f c) { return __builtin_amdgcn_mfma_f32_16x16x16bf16_1k(a, b, c, 0, 0, 0); }

template <class T> __device__ __forceinline__ void pin(T& v) { asm volatile("" : "+v"(v)); }
__device__ __forceinline__ void lgkm0() { asm volatile("s_waitcnt lgkmcnt(0)" ::: "memory"); }
template <int N> __device__ __forceinline__ void vmwait() { asm volatile("s_waitcnt vmcnt(%0)" ::"n"(N) : "memory"); }
__device__ __forceinline__ void cbar() { asm volatile("" ::: "memory"); }
__device__ __forceinline__ void fence() { cbar(); __builtin_amdgcn_sched_barrier(0); }
__device__ __forceinline__ void wg_barrier() { lgkm0(); __builtin_amdgcn_s_barrier(); cbar(); }

__device__ __forceinline__ void dma(rsrc_t r, u32 voff, u32 soff, u32 lds) {
  asm volatile("s_mov_b32 m0, %0\n\ts_nop 0\n\tbuffer_load_dwordx4 %1, %2, %3 offen nt lds" ::"s"(lds), "v"(voff), "s"(r), "s"(soff) : "memory");
}

struct Fl { u32 fl, one; };
template <u32 F> __device__ __forceinline__ void signal(const Fl& f) {
  asm volatile("s_mov_b64 exec, 1\n\tds_add_u32 %0, %1 offset:%2\n\ts_mov_b64 exec, -1" ::"v"(f.fl), "v"(f.one), "n"(F - L_FL) : "memory");
}
template <u32 F, u32 N, int SL = 0> __device__ __forceinline__ void wait_ge(const Fl& f) {
  u32 v, c, n;
  if constexpr (SL == 0)
    asm volatile("s_mov_b32 %[c], 0x400000\n"
                 ".Lw%=:\n\tds_read_b32 %[v], %[a] offset:%[o]\n\ts_sub_u32 %[c], %[c], 1\n\ts_cselect_b32 %[n], 0, %[N]\n\t"
                 "s_waitcnt lgkmcnt(0)\n\tv_cmp_gt_u32 vcc, %[n], %[v]\n\ts_cbranch_vccnz .Lw%="
                 : [v] "=&v"(v), [c] "=&s"(c), [n] "=&s"(n) : [a] "v"(f.fl), [o] "n"(F - L_FL), [N] "n"(N) : "vcc", "scc", "memory");
  else
    asm volatile("s_mov_b32 %[c], 0x400000\n"
                 ".Lw%=:\n\tds_read_b32 %[v], %[a] offset:%[o]\n\ts_sub_u32 %[c], %[c], 1\n\ts_cselect_b32 %[n], 0, %[N]\n\ts_sleep %[sl]\n\t"
                 "s_waitcnt lgkmcnt(0)\n\tv_cmp_gt_u32 vcc, %[n], %[v]\n\ts_cbranch_vccnz .Lw%="
                 : [v] "=&v"(v), [c] "=&s"(c), [n] "=&s"(n) : [a] "v"(f.fl), [o] "n"(F - L_FL), [N] "n"(N), [sl] "n"(SL) : "vcc", "scc", "memory");
}

__device__ __forceinline__ v4u gld(rsrc_t r, u32 off) { return __builtin_amdgcn_raw_buffer_load_b128(r, off, 0, 16); }
__device__ __forceinline__ void gst(rsrc_t r, u32 off, v4u v) { __builtin_amdgcn_raw_buffer_store_b128(v, r, off, 0, 16); }
__device__ __forceinline__ void gst1(rsrc_t r, u32 off, u32 v) { __builtin_amdgcn_raw_buffer_store_b32(v, r, off, 0, 16); }
struct Tag { u32 m, t; };
__device__ __forceinline__ Tag mktag(u32 t, u32 m) { Tag g = {m, t}; asm volatile("" : "+s"(g.m), "+v"(g.t)); return g; }
__device__ __forceinline__ u32 ins_tag(u32 w, const Tag& g) { u32 r; asm("v_bfi_b32 %0, %1, %2, %3" : "=v"(r) : "s"(g.m), "v"(g.t), "v"(w)); return r; }
__device__ __forceinline__ u32 pack1(float v, const Tag& g) { return ins_tag(__float_as_uint(v) + 0x200u, g); }
__device__ __forceinline__ u32 pack1q(float v, const Tag& g) { return ins_tag(__float_as_uint(v) + 0x80u, g); }
__device__ __forceinline__ v4u pack4(v4f v, const Tag& g) { return (v4u){pack1(v[0], g), pack1(v[1], g), pack1(v[2], g), pack1(v[3], g)}; }

__device__ __forceinline__ void gld_masked(v4u& w, u64 lanes, u32 off, rsrc_t r) {
  asm volatile("s_mov_b64 exec, %1\n\tbuffer_load_dwordx4 %0, %2, %3, 0 offen sc1\n\ts_mov_b64 exec, -1" : "+v"(w) : "s"(lanes), "v"(off), "s"(r) : "memory");
}
__device__ __forceinline__ void gld_first(v4u& w, u32 off, rsrc_t r) {
  asm volatile("buffer_load_dwordx4 %0, %1, %2, 0 offen sc1" : "=&v"(w) : "v"(off), "s"(r) : "memory");
}
template <int N> __device__ __forceinline__ void vm_arrive(v4u& w) { asm volatile("s_waitcnt vmcnt(%1)" : "+v"(w) : "n"(N) : "memory"); }
template <int N, u32 M, class F, class G = void (*)()>
__device__ __forceinline__ void poll_lanes(rsrc_t r, const u32 (&off)[N], u32 t, F&& use, G&& mid) {
  v4u w[N];
  u64 pend[N];
  static_for<N>([&](auto i_) {
    constexpr int i = decltype(i_)::value;
    gld_first(w[i], off[i], r);
  });
  mid();
  u32 budget = SPIN_MAX;
  for (;;) {
    u32 mk;
    asm volatile("s_sub_u32 %0, %0, 1\n\ts_cselect_b32 %1, 0, %2" : "+s"(budget), "=s"(mk) : "n"(M) : "scc");
    u64 any = 0;
    static_for<N>([&](auto i_) {
      constexpr int i = decltype(i_)::value;
      vm_arrive<N - 1 - i>(w[i]);
      const v4u z = w[i] ^ t;
      const u32 bad = ((z.x | z.y) | (z.z | z.w)) & mk;
      pend[i] = __builtin_amdgcn_ballot_w64(bad != 0u);
      any |= pend[i];
      use(i_, __builtin_bit_cast(v4f, z));
    });
    if (!any) break;
    static_for<N>([&](auto i_) {
      constexpr int i = decltype(i_)::value;
      gld_masked(w[i], pend[i], off[i], r);
    });
  }
}
template <int N, u32 M, class F> __device__ __forceinline__ void poll_lanes(rsrc_t r, const u32 (&off)[N], u32 t, F&& use) {
  poll_lanes<N, M>(r, off, t, use, []() {});
}

#define STAMP(i) do { } while (0)
#define STAMP_DECL do { } while (0)
#define STAMP_FLUSH() do { } while (0)

__device__ __forceinline__ u32 t128_tr(int lane, int t, int nb) {
  const int g = lane >> 4, q = (lane & 15) >> 2, p = lane & 3;
  const int r = 8 * g + 4 * t + q;
  const int f = (q >> 1) | (((t ^ g) & 1) << 1);
  return (u32)(r * 128 + ((nb ^ f) << 5) + ((p >> 1) << 4) + ((p & 1) << 3));
}
__device__ __forceinline__ u32 t128_tr16(int lane, int nb) {
  const int g = lane >> 4, q = (lane & 15) >> 2, p = lane & 3;
  const int r = 4 * g + q;
  const int f = (q >> 1) | (((g ^ (g >> 1)) & 1) << 1);
  return (u32)(r * 128 + ((nb ^ f) << 5) + ((p >> 1) << 4) + ((p & 1) << 3));
}
__device__ __forceinline__ u32 off_b(u32 row, u32 ch) { return 256u * row + 16u * (ch ^ (((row & 3) << 2) | ((row >> 2) & 3))); }

constexpr int N_WQ = 5, N_K0 = 6, N_K = 13, N_V1 = 17, N_V2 = 21;
template <int WIN_, int NT_, int WT_, int RAMP_, int GATE_, int GK_> struct Sched {
  static constexpr int NT = NT_, RAMP = RAMP_, WT = WT_, GATE = GATE_, GK = GK_;
  static constexpr int win(int n) { return n <= N_V2 + RAMP_ ? WIN_ : (WIN_ + (n - N_V2 - RAMP_) < WT_ ? WIN_ + (n - N_V2 - RAMP_) : WT_); }
  static constexpr int by_win(int n) { return n < NBURST ? 0 : (n - win(n) > 0 ? n - win(n) : 0); }
  static constexpr bool vgate = GATE_ >= 0 && GK_ == 0;
  static constexpr int landed(int n) { return vgate && n > N_V2 + GATE_ && by_win(n) < N_V2 ? N_V2 : by_win(n); }
  static constexpr int known(int n) { return vgate && n >= N_V2 + GATE_ && by_win(n) < N_V2 ? N_V2 : by_win(n); }
};
template <class C, int n> __device__ __forceinline__ void after(const Fl& f) {
  if constexpr (n >= NBURST && n > C::win(n) && C::landed(n) > C::known(n - 1)) vmwait<C::win(n)>();
  constexpr int a = C::known(n - 1), b = C::landed(n);
  if constexpr (a < N_WQ && b >= N_WQ) signal<F_WQ>(f);
  if constexpr (a < N_K0 && b >= N_K0) signal<F_K0>(f);
  if constexpr (a < N_K && b >= N_K) signal<F_K>(f);
  if constexpr (a < N_V1 && b >= N_V1) signal<F_V1>(f);
  if constexpr (a < N_V2 && b >= N_V2) signal<F_V2>(f);
}

__device__ __forceinline__ void* mkptr(u32 lo, u32 hi) { return (void*)(((unsigned long long)hi << 32) | lo); }

__global__ void __attribute__((aligned(KALIGN))) __launch_bounds__(768, 3) fused_decode_kernel(Args) {
  extern __shared__ __attribute__((aligned(16))) unsigned char smem[];
  v16u ka; v2u kb;
  asm volatile("s_load_dwordx16 %0, %2, 0x0\n\ts_load_dwordx2 %1, %2, 0x40" : "=&s"(ka), "=&s"(kb) : "s"(__builtin_amdgcn_kernarg_segment_ptr()));
  __builtin_amdgcn_sched_barrier(0);
  const int tid = threadIdx.x, lane = tid & 63;
  const int wv = __builtin_amdgcn_readfirstlane(tid >> 6);
  const int b = blockIdx.x, x = b & 7, j = b >> 3;
  const int g = 2 * x + (j >> 4), s = j & 15;
  const int role = j < 9 ? 0 : (j < 27 ? 1 : 3);
  const int jj = j;
  const int mi = 18 * x + j - 9;
  const int rr = (j - 27) * 8 + x;
  const int g16 = lane >> 4, c16 = lane & 15;
  STAMP_DECL;
  STAMP(0);
  u32 xres;

  const u32 vt = (u32)(((lane & 7) ^ (((lane >> 4) & 1) << 1) ^ (((lane >> 5) & 1) << 2)) * 16);
  v4u xa; v2u xb, xq;
  u32 ep, keep = 0;
  if (wv >= 10) {
    const u32 idx = (u32)(wv - 10) * 64u + (u32)lane;
    const u32 one = idx < 72u ? 0x3f803f80u : 0u;
    lds_st<v4u>(idx < 80u ? L_ONE + 16u * idx : L_FL - 1280u + 16u * min(idx, 91u), (v4u){one, one, one, one});
  }
  u32 xo0 = (u32)lane * 24u, xo1 = (u32)((s & 3) * 288 + lane * 8);
  u32 qvoff = (u32)(lane >> 3) * 8192u + vt;
  u32 qbase = (u32)(s & 3) * 144u * 8192u + (u32)(g * 256 + (s >> 2) * 64) * 2u;
  Fl fl = {L_FL, 1u};
  asm volatile("" : "+v"(xo0), "+v"(xo1), "+v"(qvoff), "+v"(fl.fl), "+v"(fl.one));
  asm volatile("" : "+s"(qbase));
  asm volatile("s_waitcnt lgkmcnt(0)" : "+s"(ka), "+s"(kb));
  const Args a = {mkptr(ka[0], ka[1]), mkptr(ka[2], ka[3]), mkptr(ka[4], ka[5]), mkptr(ka[6], ka[7]), mkptr(ka[8], ka[9]),
                  mkptr(ka[10], ka[11]), mkptr(ka[12], ka[13]), mkptr(ka[14], ka[15]), mkptr(kb[0], kb[1])};
  const rsrc_t rws = __builtin_amdgcn_make_buffer_rsrc(a.ws, 0, WS_END, 0x00020000);
  const rsrc_t rx = __builtin_amdgcn_make_buffer_rsrc((void*)a.x, 0, 1152, 0x00020000);
  if (wv == 4) {
    asm volatile("buffer_load_dwordx4 %0, %3, %5, 0 offen\n\tbuffer_load_dwordx2 %1, %3, %5, 0 offen offset:16\n\tbuffer_load_dwordx2 %2, %4, %5, 0 offen"
                 : "=&v"(xa), "=&v"(xb), "=&v"(xq) : "v"(xo0), "v"(xo1), "s"(rx) : "memory");
  }
  STAMP(14);
  wg_barrier();
  STAMP(15);
  if (wv < 4) {
    const int lw = wv;
    STAMP(5);
    const rsrc_t rq = __builtin_amdgcn_make_buffer_rsrc((void*)a.wq, 0, 576 * 4096 * 2, 0x00020000);
    static_for<5>([&](auto k_) {
      constexpr int k = decltype(k_)::value;
      const u32 i = (u32)min(lw + 4 * k, 17);
      dma(rq, qvoff ^ ((i & 1u) << 6), qbase + i * 65536u, L_WQ + i * 1024u);
    });
    STAMP(6);
  }
  {
    const bool r1 = role == 1;
    const u32 xo = ((u32)(r1 ? 64 * (wv >= 4 ? wv - 4 : 8) : 16 * rr) + 4u * (u32)(lane & (r1 ? 15 : 3)) + (u32)g16) * 2u;
    asm volatile("buffer_load_dword %0, off, %3, 0\n\tbuffer_load_ushort %1, %2, %4, 0 offen" : "=&v"(ep), "=&v"(xres) : "v"(xo), "s"(rws), "s"(rx) : "memory");
  }

  auto gather_h = [&](const int cw, const u32 t10, auto&& mid) {
    u32 va = L_VEC + (u32)(64 * cw + 4 * c16 + g16) * 2u, sa = L_ST + (u32)(lane >> 5) * 64u + (u32)cw * 4u;
    u32 off[2];
    off[0] = WS_HP + (u32)x * (8u * 2304u) + (u32)g16 * 2304u + (u32)(64 * cw + 4 * c16) * 4u;
    off[1] = off[0] + 4u * 2304u;
    float xr = bf_lo(xres);
    pin(va); pin(sa); pin(off[0]); pin(off[1]); pin(xr);
    v4f y;
    poll_lanes<2, 0x3ffu>(rws, off, t10, [&](auto i_, v4f w) { if (decltype(i_)::value == 0) y = w; else { y += w; pin(y); } }, mid);
    STAMP(9);
    const float pre = xr + rbf(rs4(y));
    const u32 hb = pk_bf(pre, pre);
    lds_st<unsigned short>(va, (unsigned short)hb);
    const float h = bf_lo(hb);
    const float sr = r16_sum(x16_sum(sw32_add(h, h * h)));
    if ((lane & 31) == 0) lds_st<float>(sa, sr);
  };
  auto load_w2 = [&](v8b (&B2)[3]) {
    const u32 a0 = L_S2 + (u32)(8 * g16 + (c16 >> 2)) * 1152u + (u32)(lane & 3) * 8u;
#pragma unroll
    for (int c = 0; c < 3; ++c) {
      const u32 o = (u32)(((3 * wv + c) ^ g16) * 32);
      B2[c] = cat8(lds_tr(a0 + o), lds_tr(a0 + 4 * 1152 + o));
    }
    asm volatile("" : "+v"(B2[0]), "+v"(B2[1]), "+v"(B2[2]));
  };
  struct Out2 { u32 ga, fa, m1, m2; };
  auto prep_out = [&]() {
    Out2 o = {L_G + (u32)g16 * 16u, g16 < 3 ? WS_FP + (u32)(3 * wv + g16) * 9216u + (u32)mi * 64u + (u32)c16 * 4u : OOB,
              0u - (u32)(g16 & 1), 0u - (u32)(g16 >> 1)};
    pin(o.ga); pin(o.fa); pin(o.m1); pin(o.m2);
    return o;
  };
  auto mlp_out = [&](const Out2& o, const v8b (&B2)[3], const Tag& tg) {
    wg_barrier();
    STAMP(12);
    const v8b A = lds_frag(o.ga);
    const v4f z = {0.f, 0.f, 0.f, 0.f};
    const v4f d0 = mfma32(A, B2[0], z), d1 = mfma32(A, B2[1], z), d2 = mfma32(A, B2[2], z);
    const u32 v = bfi(o.m2, __float_as_uint(d2[0]), bfi(o.m1, __float_as_uint(d1[0]), __float_as_uint(d0[0])));
    gst1(rws, o.fa, pack1(__uint_as_float(v), tg));
  };
  auto total = [](v4f v) { float r = rs4(v); r += dppf<0x124>(r); r += dppf<0x128>(r); return r; };
  auto reduce_part = [&](const int cw, const u32 t10) {
    const u32 col = (u32)(4 * (c16 & 3) + g16);
    u32 wa = c16 < 4 ? L_VEC + col * 48u + (u32)cw * 4u : L_VEC + 1024u + (u32)lane * 4u;
    u32 off[1] = {WS_FP + (u32)rr * 9216u + (u32)cw * 1024u + (u32)lane * 16u};
    pin(wa); pin(off[0]);
    v4f y;
    poll_lanes<1, 0x3ffu>(rws, off, t10, [&](auto, v4f w) { y = w; });
    STAMP(9);
    lds_st<float>(wa, total(y));
  };

  if (__builtin_expect(wv < 4, 1)) {
    const int lw = wv;
    v8b B[16];
    const u32 vkv = ((u32)(lane >> 4) * 256u + (u32)(((lane & 15) ^ ((lane >> 4) << 2)) * 16)) ^ (u32)(lw << 4);
    const u32 bkv = (u32)(g * 8192 + 6144 + s * 128) * 256u + lw * 1024u;
    const rsrc_t rk = __builtin_amdgcn_make_buffer_rsrc((void*)a.kc, 0, 16 * 8192 * 256, 0x00020000);
    auto stream = [&](auto c_) {
      using C = decltype(c_);
      static_for<8>([&](auto k_) {
        constexpr int k = decltype(k_)::value;
        dma(rk, vkv, bkv + k * 4096u, L_K + lw * 1024u + k * 4096u);
        after<C, N_WQ + k + 1>(fl);
      });
      STAMP(1);
      const rsrc_t rv = __builtin_amdgcn_make_buffer_rsrc((void*)a.vc, 0, 16 * 8192 * 256, 0x00020000);
      static_for<8>([&](auto k_) {
        constexpr int k = decltype(k_)::value;
        dma(rv, vkv, bkv + k * 4096u, L_V + lw * 1024u + k * 4096u);
        after<C, N_K + k + 1>(fl);
      });
      STAMP(2);
      fence();
      asm volatile("" : "+v"(ep), "+v"(xres));
      if constexpr (C::NT == 0) {
        static_assert(C::landed(N_V2) >= N_V1, "first half of V is published by the window");
        vmwait<0>();
        signal<F_V2>(fl);
      } else {
        static_assert(C::landed(N_V2 + C::NT) >= N_V2, "V is published inside the tile phase");
        constexpr bool WO = C::NT == 16;
        int lo_ = lane;
        asm volatile("" : "+v"(lo_));
        const rsrc_t rw = __builtin_amdgcn_make_buffer_rsrc((void*)(WO ? a.wo : a.w1), 0, WO ? 4096u * 576u * 2u : 4608u * 576u * 2u, 0x00020000);
        const rsrc_t rw2 = __builtin_amdgcn_make_buffer_rsrc((void*)a.w2, 0, 4608u * 576u * 2u, 0x00020000);
        const u32 wbase = WO ? (u32)x * 512u * 1152u + (u32)jj * 128u : (u32)mi * 64u + (u32)lw * 16u * 9216u;
        const u32 wvoff = WO ? ((u32)(lo_ >> 3) * 1152u + vt) ^ (u32)((lw & 1) << 6)
                             : (u32)(lo_ >> 2) * 9216u + (u32)(((lo_ & 3) ^ (lo_ >> 5) ^ ((lw & 1) << 1)) * 16);
        const u32 w2base = (u32)mi * 36864u + (u32)lw * 9216u, w2voff = (u32)(lo_ * 16) ^ (u32)(lw << 5);
        static_for<C::NT>([&](auto k_) {
          constexpr int k = decltype(k_)::value;
          const u32 i = lw + 4 * k;
          if constexpr (C::GATE == k && C::GK == 0) {
            if constexpr (C::landed(N_V2 + k) < N_V2) {
              if constexpr (C::landed(N_V2 + k) < N_V1) { vmwait<N_V2 + k - N_V1>(); signal<F_V1>(fl); }
              vmwait<k>();
              signal<F_V2>(fl);
            }
            wait_ge<F_O, GATE_O, 1>(fl);
          }
          if constexpr (C::GATE == k && C::GK == 1) wait_ge<F_Q, 1, 1>(fl);
          if constexpr (C::NT == 16) dma(rw, wvoff, wbase + i * (8u * 1152u), L_S + i * 1024u);
          else if constexpr (k < 9) dma(rw, wvoff, wbase + k * (64u * 9216u), L_S + i * 1024u);
          else dma(rw2, w2voff, w2base + (k - 9) * 1024u, L_S2 + lw * 9216u + (k - 9) * 1024u);
          after<C, N_V2 + k + 1>(fl);
        });
        constexpr int NH = C::NT / 2;
        vmwait<(C::NT - NH < C::win(N_V2 + C::NT) ? C::NT - NH : C::win(N_V2 + C::NT))>();
        signal<F_S1>(fl);
        wait_ge<F_S1, 4, 1>(fl);
        if constexpr (C::NT == 16) {
          const u32 a0 = L_S + t128_tr(lane, 0, lw), a1 = L_S + t128_tr(lane, 1, lw);
#pragma unroll
          for (int c = 0; c < NH; ++c) B[c] = cat8(lds_tr(a0 + c * 4096), lds_tr(a1 + c * 4096));
          vmwait<0>();
          signal<F_S2>(fl);
          STAMP(3);
          wait_ge<F_S2, 4, 1>(fl);
#pragma unroll
          for (int c = NH; c < C::NT; ++c) B[c] = cat8(lds_tr(a0 + c * 4096), lds_tr(a1 + c * 4096));
        } else {
          const u32 a0 = L_S + (u32)(((lane >> 1) & 1) * 288 + 8 * g16 + (c16 >> 2)) * 64u + (u32)((lw ^ g16) << 4) + (u32)(lane & 1) * 8u;
#pragma unroll
          for (int c = 0; c < 9; ++c) B[c] = cat8(lds_tr(a0 + c * 2048), lds_tr(a0 + 256 + c * 2048));
          vmwait<MLAG>();
          signal<F_S3>(fl);
        }
      }
    };
#define RD(r, o) "ds_read_b128 %[a" #r "], %[ab] offset:" #o "\n\t"
#define WT(n) "s_waitcnt lgkmcnt(" #n ")\n\t"
#define MF0(r, b) "v_mfma_f32_16x16x32_bf16 %[acc], %[a" #r "], %[b" #b "], 0\n\t"
#define MF(r, b) "v_mfma_f32_16x16x32_bf16 %[acc], %[a" #r "], %[b" #b "], %[acc]\n\t"
#define CH_OUT [acc] "=&v"(acc), [a0] "=&v"(A0), [a1] "=&v"(A1), [a2] "=&v"(A2), [a3] "=&v"(A3), [a4] "=&v"(A4), [a5] "=&v"(A5), [a6] "=&v"(A6)
#define CH_IN16 [ab] "v"(ab), [b0] "v"(B[0]), [b1] "v"(B[1]), [b2] "v"(B[2]), [b3] "v"(B[3]), [b4] "v"(B[4]), [b5] "v"(B[5]), [b6] "v"(B[6]), \
                [b7] "v"(B[7]), [b8] "v"(B[8]), [b9] "v"(B[9]), [b10] "v"(B[10]), [b11] "v"(B[11]), [b12] "v"(B[12]), [b13] "v"(B[13]), \
                [b14] "v"(B[14]), [b15] "v"(B[15])
    v4f acc;
    v4u A0, A1, A2, A3, A4, A5, A6, A7, A8;
    if (role == 0) {
      stream(Sched<WIN_W, 16, WT_W, 0, GATE_W, 1>{});
      const u32 t10 = (__builtin_amdgcn_readfirstlane(ep) + 1u) & 0x3ffu;
      u32 ab = L_VEC + (u32)g16 * 16u;
      u32 o0 = WS_HP + x * 2304u + (u32)(64 * jj + 16 * wv + c16) * 4u + (u32)g16 * (8u * 2304u), o1 = o0 + 4u * 8u * 2304u;
      asm volatile("" : "+v"(ab), "+v"(o0), "+v"(o1));
      const Tag tg = mktag(t10, 0x3ffu);
      STAMP(4);
      wg_barrier();
      STAMP(10);
      asm volatile(""
                   RD(0, 0) RD(1, 64) RD(2, 128) RD(3, 192) RD(4, 256) RD(5, 320) RD(6, 384) WT(6) MF0(0, 0) RD(0, 448) WT(6)
                   MF(1, 1) RD(1, 512) WT(6) MF(2, 2) RD(2, 576) WT(6) MF(3, 3) RD(3, 640) WT(6) MF(4, 4) RD(4, 704) WT(6)
                   MF(5, 5) RD(5, 768) WT(6) MF(6, 6) RD(6, 832) WT(6) MF(0, 7) RD(0, 896) WT(6) MF(1, 8) RD(1, 960) WT(6)
                   MF(2, 9) WT(5) MF(3, 10) WT(4) MF(4, 11) WT(3) MF(5, 12) WT(2) MF(6, 13) WT(1) MF(0, 14) WT(0) MF(1, 15)
                   "s_nop 7"
                   : CH_OUT : CH_IN16 : "memory");
      const u32 hw = pack1(acc[0], tg);
      gst1(rws, o0, hw);
      gst1(rws, o1, hw);
      STAMP(11);
      STAMP_FLUSH();
      return;
    }
    if (role == 1) {
      stream(Sched<WIN, 18, WT_M, RAMP_M, GATE_M, MGK>{});
      const u32 t10 = (__builtin_amdgcn_readfirstlane(ep) + 1u) & 0x3ffu;
      STAMP(4);
      v8b B2[3];
      Out2 o2;
      const Tag tg = mktag(t10, 0x3ffu);
      u32 ab = ((c16 & 2) ? L_ONE : L_VEC) + (u32)(c16 & 1) * 576u + (u32)g16 * 16u;
      u32 ga = lane < 8 ? L_G + (u32)(8 * wv + lane) * 2u : L_VEC + 1152u + (u32)lane * 2u;
      u32 sa = L_ST + (u32)(lane & 31) * 4u;
      asm volatile("" : "+v"(ab), "+v"(ga), "+v"(sa));
      auto finish = [&](auto n_) {
        vmwait<decltype(n_)::value>();
        signal<F_S2>(fl);
        STAMP(3);
        wait_ge<F_S2, 4, 1>(fl);
        load_w2(B2);
        o2 = prep_out();
      };
      if (lw == 0) gather_h(8, t10, [&]() { finish(std::integral_constant<int, 2>{}); });
      else finish(std::integral_constant<int, 0>{});
      float mean, rden;
      wg_barrier();
      STAMP(10);
      asm volatile("ds_read_b32 %[mean], %[sa]\n\t"
                   RD(0, 0) RD(1, 64) RD(2, 128) RD(3, 192) RD(4, 256) RD(5, 320) RD(6, 384) RD(7, 448) RD(8, 512)
                   "s_waitcnt lgkmcnt(9)\n\t"
                   "v_add_f32_dpp %[mean], %[mean], %[mean] quad_perm:[1,0,3,2] row_mask:0xf bank_mask:0xf bound_ctrl:1\n\t"
                   WT(8) MF0(0, 0)
                   "v_add_f32_dpp %[mean], %[mean], %[mean] quad_perm:[2,3,0,1] row_mask:0xf bank_mask:0xf bound_ctrl:1\n\t"
                   WT(7) MF(1, 1)
                   "v_add_f32_dpp %[mean], %[mean], %[mean] row_half_mirror row_mask:0xf bank_mask:0xf bound_ctrl:1\n\t"
                   WT(6) MF(2, 2)
                   "v_add_f32_dpp %[mean], %[mean], %[mean] row_mirror row_mask:0xf bank_mask:0xf bound_ctrl:1\n\t"
                   "v_mov_b32 %[rden], %[mean]\n\t"
                   WT(5) MF(3, 3)
                   "v_permlane16_swap_b32 %[mean], %[rden]\n\t"
                   "v_mul_f32 %[mean], 0x3ae38e39, %[mean]\n\t"
                   "v_mul_f32 %[rden], 0x3ae38e39, %[rden]\n\t"
                   WT(4) MF(4, 4)
                   "v_fma_f32 %[rden], -%[mean], %[mean], %[rden]\n\t"
                   "v_max_f32 %[rden], 0, %[rden]\n\t"
                   "v_cvt_pk_bf16_f32 %[rden], %[rden], %[rden]\n\t"
                   WT(3) MF(5, 5)
                   "v_lshlrev_b32 %[rden], 16, %[rden]\n\t"
                   "v_add_f32 %[rden], 0x3727c5ac, %[rden]\n\t"
                   "v_cvt_pk_bf16_f32 %[rden], %[rden], %[rden]\n\t"
                   WT(2) MF(6, 6)
                   "v_lshlrev_b32 %[rden], 16, %[rden]\n\t"
                   "v_sqrt_f32 %[rden], %[rden]\n\t"
                   "v_cvt_pk_bf16_f32 %[mean], %[mean], %[mean]\n\t"
                   WT(1) MF(7, 7)
                   "v_cvt_pk_bf16_f32 %[rden], %[rden], %[rden]\n\t"
                   "v_lshlrev_b32 %[mean], 16, %[mean]\n\t"
                   "v_lshlrev_b32 %[rden], 16, %[rden]\n\t"
                   WT(0) MF(8, 8)
                   "v_rcp_f32 %[rden], %[rden]\n\t"
                   "s_nop 7"
                   : CH_OUT, [a7] "=&v"(A7), [a8] "=&v"(A8), [mean] "=&v"(mean), [rden] "=&v"(rden)
                   : [ab] "v"(ab), [b0] "v"(B[0]), [b1] "v"(B[1]), [b2] "v"(B[2]), [b3] "v"(B[3]), [b4] "v"(B[4]), [b5] "v"(B[5]),
                     [b6] "v"(B[6]), [b7] "v"(B[7]), [b8] "v"(B[8]), [sa] "v"(sa) : "memory");
      const float dot = acc[0] + dppf<0x128>(acc[1]), cs = acc[2] + dppf<0x128>(acc[3]);
      const float pre = rbf((dot - mean * cs) * rden);
      const float gv = pre * __builtin_amdgcn_rcpf(1.0f + __builtin_amdgcn_exp2f(pre * -1.4426950408889634f));
      lds_st<unsigned short>(ga, (unsigned short)pk_bf(gv, gv));
      mlp_out(o2, B2, tg);
      STAMP(11);
      STAMP_FLUSH();
      return;
    }
    stream(Sched<WIN, 0, WIN, 0, -1, 0>{});
    if (lw != 0 || rr >= 36) { STAMP_FLUSH(); return; }
    reduce_part(8, (__builtin_amdgcn_readfirstlane(ep) + 1u) & 0x3ffu);
    wg_barrier();
    STAMP_FLUSH();
    return;
  }

  const int cw = wv - 4;
  if (role == 3 && cw == 0) keep = __builtin_amdgcn_raw_buffer_load_b32(__builtin_amdgcn_make_buffer_rsrc(a.out, 0, 1152, 0x00020000), 0, 0, 0);
  if (cw == 0) {
    float v[12];
    asm volatile("s_waitcnt vmcnt(2)" : "+v"(xa), "+v"(xb), "+v"(xq)::"memory");
    STAMP(13);
#pragma unroll
    for (int i = 0; i < 4; ++i) { v[2 * i] = bf_lo(xa[i]); v[2 * i + 1] = bf_hi(xa[i]); }
#pragma unroll
    for (int i = 0; i < 2; ++i) { v[8 + 2 * i] = bf_lo(xb[i]); v[9 + 2 * i] = bf_hi(xb[i]); }
    const float x0 = __uint_as_float(__builtin_amdgcn_readfirstlane(__float_as_uint(v[0])));
    float s1 = 0.f, s2 = 0.f;
#pragma unroll
    for (int i = 0; i < 12; ++i) { const float d = v[i] - x0; s1 += d; s2 += d * d; }
    if (lane >= 48) { s1 = 0.f; s2 = 0.f; }
    const float sr = r16_sum(x16_sum(sw32_add(s1, s2)));
    auto r2 = __builtin_amdgcn_permlane32_swap(__float_as_uint(sr), __float_as_uint(sr), false, false);
    const float md = __uint_as_float(r2[0]) * (1.0f / 576.0f);
    const float var = fmaxf(__uint_as_float(r2[1]) * (1.0f / 576.0f) - md * md, 0.f);
    const float m = rbf(x0 + md);
    const float den = rbf(__builtin_amdgcn_sqrtf(rbf(rbf(var) + 1e-5f)));
    const float rden = __builtin_amdgcn_rcpf(den);
    v2u n;
#pragma unroll
    for (int i = 0; i < 2; ++i) {
      const u32 d = pk_bf(bf_lo(xq[i]) - m, bf_hi(xq[i]) - m);
      n[i] = pk_bf(bf_lo(d) * rden, bf_hi(d) * rden);
    }
    if (lane < 36) lds_st<v2u>(L_VEC + lane * 8, n);
    signal<F_X>(fl);
    STAMP(1);
  }
  asm volatile("" ::"v"(keep));
  vmwait<0>();
  fence();
  asm volatile("" : "+v"(ep), "+v"(xres));
  const u32 tag32 = __builtin_amdgcn_readfirstlane(ep) + 1u;
  const u32 t10 = tag32 & 0x3ffu, t8 = tag32 & 0xffu;
  const Tag tg = mktag(t10, 0x3ffu);
  if (cw < 4) {
    const Tag tq = mktag(t8, 0xffu);
    u32 b0 = L_WQ + t128_tr(lane, 0, cw), b1 = L_WQ + t128_tr(lane, 1, cw), b2 = L_WQ + 16384 + t128_tr16(lane, cw);
    u32 aa = L_VEC + (u32)g16 * 16u;
    u32 qo = lane < 16 ? WS_QB + g * 4096u + (u32)(s & 3) * 1024u + (u32)((s >> 2) * 64 + cw * 16 + c16) * 4u : OOB;
    pin(b0); pin(b1); pin(b2); pin(aa); pin(qo);
    wait_ge<F_X, 1, 1>(fl);
    v8b A[4];
#pragma unroll
    for (int c = 0; c < 4; ++c) A[c] = lds_frag(aa + c * 64);
    v4s A4 = lds_ld<v4s>(aa + 256 - g16 * 8);
    pin(A[0]); pin(A[1]); pin(A[2]); pin(A[3]); pin(A4);
    wait_ge<F_WQ, 4>(fl);
    STAMP(2);
    v8b Bq[4];
    const v4s B4 = lds_tr(b2);
#pragma unroll
    for (int c = 0; c < 4; ++c) Bq[c] = cat8(lds_tr(b0 + c * 4096), lds_tr(b1 + c * 4096));
    fence();
    v4f acc = {0.f, 0.f, 0.f, 0.f};
    const v4f tail = mfma16(A4, B4, acc);
#pragma unroll
    for (int c = 0; c < 4; ++c) acc = mfma32(A[c], Bq[c], acc);
    gst1(rws, qo, pack1q(acc[0] + tail[0], tq));
    signal<F_QD>(fl);
    STAMP(3);
  }
  const int tile = (cw + 4) & 7;
  u32 qa = L_Q + (u32)(lane & 1) * 272u + (u32)g16 * 16u;
  u32 swa = L_SC + (u32)((((tile & 1) << 1) | (g16 >> 1)) * 16 + (c16 & 1) + 2 * (tile >> 1) + 8 * (g16 & 1)) * 16u + (u32)((c16 >> 1) & 3) * 4u;
  u32 sra = L_SC + (u32)lane * 16u;
  u32 va0 = L_V + off_b(8 * g16 + (c16 >> 2), 2 * cw + ((lane & 3) >> 1)) + 8 * (lane & 1);
  u32 va1 = L_V + off_b(8 * g16 + (c16 >> 2) + 4, 2 * cw + ((lane & 3) >> 1)) + 8 * (lane & 1);
  u32 oa = c16 < 2 ? WS_OP + (u32)(g * 16 + s) * OPS + (u32)c16 * 512u + (u32)(16 * cw + 4 * g16) * 4u : OOB;
  u32 sta = tid == 256 ? WS_OP + (u32)(g * 16 + s) * OPS + 1024u : OOB;
  u32 sm1 = 0u - (u32)((c16 >> 1) & 1), sm2 = 0u - (u32)((c16 >> 2) & 1);
  pin(qa); pin(swa); pin(sra); pin(va0); pin(va1); pin(oa); pin(sta); pin(sm1); pin(sm2);
  v8b KA[4];
  {
    u32 kaa[4];
#pragma unroll
    for (int c = 0; c < 4; ++c) { kaa[c] = L_K + off_b(16 * tile + c16, 4 * c + g16); pin(kaa[c]); }
    if (cw == 4) wait_ge<F_K0, 4, 1>(fl); else wait_ge<F_K, 4, LSLEEP>(fl);
#pragma unroll
    for (int c = 0; c < 4; ++c) KA[c] = lds_frag(kaa[c]);
    pin(KA[0]); pin(KA[1]); pin(KA[2]); pin(KA[3]);
  }
  if (cw == 4) {
    u32 off[4], wa = L_Q + (u32)(lane >> 5) * 272u + (u32)(lane & 31) * 8u;
#pragma unroll
    for (int i = 0; i < 4; ++i) { off[i] = WS_QB + g * 4096u + lane * 16u + i * 1024u; pin(off[i]); }
    pin(wa);
    wait_ge<F_QD, 4, 1>(fl);
    if (QDELAY) __builtin_amdgcn_s_sleep(QDELAY);
    STAMP(8);
    v4f q0;
    poll_lanes<4, 0xffu>(rws, off, t8, [&](auto i_, v4f w) {
      constexpr int i = decltype(i_)::value;
      if (i == 0) q0 = w; else { q0 += w; pin(q0); }
    });
    const v4f q = q0;
    lds_st<v2u>(wa, (v2u){pk_bf(q[0], q[1]), pk_bf(q[2], q[3])});
    signal<F_Q>(fl);
    STAMP(3);
  }
  wait_ge<F_Q, 1, LSLEEP>(fl);
  STAMP(4);
  {
    v8b Q[4];
#pragma unroll
    for (int c = 0; c < 4; ++c) Q[c] = lds_frag(qa + c * 64);
    fence();
    v4f acc = {0.f, 0.f, 0.f, 0.f};
#pragma unroll
    for (int c = 0; c < 4; ++c) acc = mfma32(KA[c], Q[c], acc);
    const u32 av = bfi(sm2, bfi(sm1, __float_as_uint(acc[3]), __float_as_uint(acc[2])), bfi(sm1, __float_as_uint(acc[1]), __float_as_uint(acc[0])));
    lds_st<float>(swa, rbf(rbf(__uint_as_float(av)) * 0.08838834764831845f));
    signal<F_SC>(fl);
  }
  v4f sc;
  {
    u32 v, c, n;
    asm volatile("s_mov_b32 %[c], 0x400000\n"
                 ".Lw%=:\n\tds_read_b32 %[v], %[a] offset:%[o]\n\tds_read_b128 %[sc], %[sra]\n\ts_sub_u32 %[c], %[c], 1\n\ts_cselect_b32 %[n], 0, 8\n\t"
                 "s_waitcnt lgkmcnt(0)\n\tv_cmp_gt_u32 vcc, %[n], %[v]\n\ts_cbranch_vccnz .Lw%="
                 : [v] "=&v"(v), [c] "=&s"(c), [n] "=&s"(n), [sc] "=&v"(sc) : [a] "v"(fl.fl), [o] "n"(F_SC - L_FL), [sra] "v"(sra) : "vcc", "scc", "memory");
  }
  STAMP(5);
  v8b VA[4];
  u32 fv;
  float m;
  {
    asm volatile("v_max3_f32 %0, %1, %2, %3\n\tv_max_f32 %0, %0, %4" : "=&v"(m) : "v"(sc[0]), "v"(sc[1]), "v"(sc[2]), "v"(sc[3]) : "memory");
    fence();
    fv = *(const volatile LAS u32*)(size_t)(fl.fl + (F_V2 - L_FL));
    fence();
    const v4s t0 = lds_tr(va0);
    fence();
    MAX_DPP(m, m, "row_ror:2");
    fence();
    const v4s t1 = lds_tr(va1), t2 = lds_tr(va0 + 8192);
    fence();
    MAX_DPP(m, m, "row_ror:4");
    fence();
    const v4s t3 = lds_tr(va1 + 8192), t4 = lds_tr(va0 + 16384);
    fence();
    float t;
    asm volatile("v_max_f32_dpp %0, %0, %0 row_ror:8 row_mask:0xf bank_mask:0xf\n\tv_mov_b32 %1, %0" : "+v"(m), "=&v"(t)::"memory");
    fence();
    const v4s t5 = lds_tr(va1 + 16384), t6 = lds_tr(va0 + 24576);
    fence();
    asm volatile("v_permlane16_swap_b32 %0, %1\n\tv_max_f32 %0, %0, %1\n\tv_mov_b32 %1, %0" : "+v"(m), "+v"(t)::"memory");
    fence();
    const v4s t7 = lds_tr(va1 + 24576);
    fence();
    asm volatile("s_nop 0\n\tv_permlane32_swap_b32 %0, %1\n\tv_max_f32 %0, %0, %1" : "+v"(m), "+v"(t)::"memory");
    fence();
    VA[0] = cat8(t0, t1); VA[1] = cat8(t2, t3); VA[2] = cat8(t4, t5); VA[3] = cat8(t6, t7);
  }
  v8b PB[4];
  float e[4];
  {
    const float mc = m * -1.4426950408889634f;
#pragma unroll
    for (int i = 0; i < 4; ++i) e[i] = __builtin_amdgcn_exp2f(__builtin_fmaf(sc[i], 1.4426950408889634f, mc));
    const u32 p0 = pk_bf(e[0], e[1]), p1 = pk_bf(e[2], e[3]);
    PB[0] = __builtin_bit_cast(v8b, (v4u){p0, p1, dppu<0x108>(p0), dppu<0x108>(p1)});
    PB[1] = __builtin_bit_cast(v8b, (v4u){dppu<0x102>(p0), dppu<0x102>(p1), dppu<0x10a>(p0), dppu<0x10a>(p1)});
    PB[2] = __builtin_bit_cast(v8b, (v4u){dppu<0x104>(p0), dppu<0x104>(p1), dppu<0x10c>(p0), dppu<0x10c>(p1)});
    PB[3] = __builtin_bit_cast(v8b, (v4u){dppu<0x106>(p0), dppu<0x106>(p1), dppu<0x10e>(p0), dppu<0x10e>(p1)});
  }
  auto stats = [&]() {
    float l = (e[0] + e[1]) + (e[2] + e[3]);
    l += dppf<0x122>(l); l += dppf<0x124>(l); l += dppf<0x128>(l);
    l = x32_sum(x16_sum(l));
    gst(rws, sta, pack4((v4f){m, l, dppf<0xB1>(m), dppf<0xB1>(l)}, tg));
  };
  {
    v4f acc = {0.f, 0.f, 0.f, 0.f};
    if (__builtin_expect(__builtin_amdgcn_ballot_w64(fv >= 4u) != 0, 1)) {
#pragma unroll
      for (int c = 0; c < 4; ++c) acc = mfma32(VA[c], PB[c], acc);
      stats();
    } else {
      stats();
      wait_ge<F_V1, 4>(fl);
#pragma unroll
      for (int c = 0; c < 2; ++c) acc = mfma32(cat8(lds_tr(va0 + c * 8192), lds_tr(va1 + c * 8192)), PB[c], acc);
      wait_ge<F_V2, 4>(fl);
      STAMP(6);
#pragma unroll
      for (int c = 2; c < 4; ++c) acc = mfma32(cat8(lds_tr(va0 + c * 8192), lds_tr(va1 + c * 8192)), PB[c], acc);
    }
    gst(rws, oa, pack4(acc, tg));
    signal<F_O>(fl);
  }
  STAMP(7);

  if (role == 0) {
    const int gg = 2 * x + (cw >> 2), hh = (cw >> 1) & 1;
    const u32 ob = WS_OP + (u32)(gg * 16 + 4 * g16) * OPS;
    u32 va = L_VEC + (u32)(64 * cw + 4 * c16 + g16) * 2u;
    u32 so[1] = {ob + (u32)(c16 & 3) * OPS + 1024u}, off[4];
#pragma unroll
    for (int i = 0; i < 4; ++i) { off[i] = ob + hh * 512u + (cw & 1) * 256u + c16 * 16u + i * OPS; pin(off[i]); }
    pin(va); pin(so[0]);
    v4f st;
    poll_lanes<1, 0x3ffu>(rws, so, t10, [&](auto, v4f w) { st = w; });
    float fq[4];
    v4f y0;
    poll_lanes<4, 0x3ffu>(rws, off, t10, [&](auto i_, v4f w) {
      constexpr int i = decltype(i_)::value;
      if (i == 0) y0 = fq[0] * w; else y0 += fq[i] * w;
      pin(y0);
    }, [&]() {
      float s0 = st[0], s1 = st[1], s2 = st[2], s3 = st[3];
      pin(s0); pin(s1); pin(s2); pin(s3);
      const float mS = hh ? s2 : s0, lS = hh ? s3 : s1;
      float M = vmax(mS, dppf<0xB1>(mS));
      M = vmax(M, dppf<0x4E>(M));
      { auto r = __builtin_amdgcn_permlane16_swap(__float_as_uint(M), __float_as_uint(M), false, false); M = vmax(__uint_as_float(r[0]), __uint_as_float(r[1])); }
      { auto r = __builtin_amdgcn_permlane32_swap(__float_as_uint(M), __float_as_uint(M), false, false); M = vmax(__uint_as_float(r[0]), __uint_as_float(r[1])); }
      const float eS = __builtin_amdgcn_exp2f((mS - M) * 1.4426950408889634f);
      float L = eS * lS;
      L += dppf<0xB1>(L); L += dppf<0x4E>(L);
      L = x32_sum(x16_sum(L));
      const float f = eS * __builtin_amdgcn_rcpf(L);
      fq[0] = dppf<0x00>(f); fq[1] = dppf<0x55>(f); fq[2] = dppf<0xAA>(f); fq[3] = dppf<0xFF>(f);
      pin(fq[0]); pin(fq[1]); pin(fq[2]); pin(fq[3]);
      STAMP(8);
    });
    STAMP(9);
    const float o = rs4(y0);
    lds_st<unsigned short>(va, (unsigned short)pk_bf(o, o));
    wg_barrier();
    STAMP(10);
    STAMP_FLUSH();
    return;
  }
  if (role == 1) {
    v8b B2[3];
    const Out2 o2 = prep_out();
    wait_ge<F_S3, 4, 1>(fl);
    gather_h(cw, t10, []() {});
    wg_barrier();
    STAMP(10);
    load_w2(B2);
    mlp_out(o2, B2, tg);
    STAMP(11);
    STAMP_FLUSH();
    return;
  }
  if (rr >= 36) { STAMP_FLUSH(); return; }
  {
    const u32 col = (u32)(4 * (c16 & 3) + g16);
    u32 ra = L_VEC + col * 48u, oo = c16 < 4 ? (u32)(16 * rr + col) * 2u : OOB;
    pin(ra); pin(oo);
    const rsrc_t ro = __builtin_amdgcn_make_buffer_rsrc(a.out, 0, 1152, 0x00020000);
    float h = 0.f;
    if (cw == 0) {
      u32 off[1] = {lane < 32 ? WS_HP + (u32)x * (8u * 2304u) + (u32)(lane >> 2) * 2304u + (u32)(16 * rr + 4 * (lane & 3)) * 4u : OOB};
      const u32 msk = lane < 32 ? 0x3ffu : 0u, tx = lane < 32 ? t10 : 0u;
      v4u w;
#pragma unroll 1
      for (u32 it = 0; it < SPIN_MAX; ++it) {
        cbar();
        w = gld(rws, off[0]) ^ tx;
        if (__builtin_amdgcn_ballot_w64(((w.x | w.y | w.z | w.w) & msk) != 0u) == 0) break;
      }
      h = rbf(bf_lo(xres) + rbf(total(__builtin_bit_cast(v4f, w))));
      pin(h);
      if (rr == 0 && lane == 0) __builtin_amdgcn_raw_buffer_store_b32(tag32, rws, WS_EP, 0, 16);
      STAMP(8);
    }
    reduce_part(cw, t10);
    wg_barrier();
    if (cw == 0) {
      const v4f p0 = lds_ld<v4f>(ra), p1 = lds_ld<v4f>(ra + 16);
      const float p2 = lds_ld<float>(ra + 32);
      const v4f ps = p0 + p1;
      const u32 o = pk_bf(h + rbf(((ps[0] + ps[1]) + (ps[2] + ps[3])) + p2), 0.f);
      __builtin_amdgcn_raw_buffer_store_b16((unsigned short)o, ro, oo, 0, 16);
    }
    STAMP(10);
    STAMP_FLUSH();
  }
}

}

extern "C" void kernel_launch(void* const* d_in, const int* in_sizes, int n_in, void* d_out, int out_size, void* d_ws, size_t ws_size,
                              hipStream_t stream) {
  (void)in_sizes; (void)n_in; (void)out_size; (void)ws_size;
  Args a;
  a.x = d_in[0]; a.kc = d_in[1]; a.vc = d_in[2]; a.wq = d_in[3]; a.wo = d_in[4]; a.w1 = d_in[6]; a.w2 = d_in[7];
  a.out = d_out; a.ws = d_ws;
  (void)hipFuncSetAttribute((const void*)fused_decode_kernel, hipFuncAttributeMaxDynamicSharedMemorySize, (int)L_END);
  hipLaunchKernelGGL(fused_decode_kernel, dim3(256), dim3(768), L_END, stream, a);
}
```
